```python
import torch

def _softmax(x, axis=-1):
    return torch.softmax(x, dim=axis)
def _gelu(x):
    return torch.nn.functional.gelu(x, approximate="tanh")
def _silu(x):
    return torch.nn.functional.silu(x)
def _sigmoid(x):
    return torch.sigmoid(x)
def _layernorm(x, axis=-1, eps=1e-5):
    m = x.mean(dim=axis, keepdim=True)
    v = x.var(dim=axis, keepdim=True, unbiased=False)
    return (x - m) / torch.sqrt(v + eps)
def _scatter_add(tgt, idx, src, axis=0):
    return tgt.clone().index_add_(axis, idx, src)
def _segment_sum(src, seg, n):
    out = torch.zeros((n,) + src.shape[1:], dtype=src.dtype)
    return out.index_add_(0, seg, src)
def _pad(x, pw):
    flat = tuple(v for pair in reversed(pw) for v in pair)
    return torch.nn.functional.pad(x, flat)

kernel_name = 'recurrent_scan__ssm_conv_s4d__dense_activation_squared_relu__prompt'

def setup_inputs():

    _w = (lambda a: (lambda t: t.to(torch.bfloat16) if t.is_floating_point() else t)(torch.as_tensor(a)))
    def _A_setup():
        Bsz, L, D, N = 1, 2048, 640, 192
        return {
            "u": _w((lambda *s: torch.randn(*s, dtype=torch.bfloat16) * 0.05)(Bsz, L, D)),
            "log_A_real": _w((lambda *s: torch.randn(*s, dtype=torch.bfloat16) * 3.2)(D, N) * 0.5),
            "A_imag": _w((lambda *s: torch.randn(*s, dtype=torch.bfloat16) * 0.05)(D, N) * 3.14),
            "C_real": _w((lambda *s: torch.randn(*s, dtype=torch.bfloat16) * 0.2)(D, N) / N ** 0.5),
            "C_imag": _w((lambda *s: torch.randn(*s, dtype=torch.bfloat16) * 0.2)(D, N) / N ** 0.5),
            "log_dt": _w((lambda *s: torch.randn(*s, dtype=torch.bfloat16) * 0.05)(D) * 0.5 - 4.0),
            "D_skip": _w((lambda *s: torch.randn(*s, dtype=torch.bfloat16) * 0.05)(D)),
        }
    _A_in = _A_setup()

    def _B_setup():
        B, S, D, F = 1, 2048, 640, 1536
        return {
            "x": _w((lambda *s: torch.randn(*s, dtype=torch.bfloat16) * 0.05)(B, S, D)),
            "w1": _w((lambda *s: torch.randn(*s, dtype=torch.bfloat16) * 0.05)(D, F)),
            "b1": _w((lambda *s: torch.randn(*s, dtype=torch.bfloat16) * 0.4)(F)),
            "w2": _w((lambda *s: torch.randn(*s, dtype=torch.bfloat16) * 0.05)(F, D)),
            "b2": _w((lambda *s: torch.randn(*s, dtype=torch.bfloat16) * 1.6)(D)),
        }
    _B_in = _B_setup()

    return {
        "A_u": _A_in["u"],
        "A_log_A_real": _A_in["log_A_real"],
        "A_A_imag": _A_in["A_imag"],
        "A_C_real": _A_in["C_real"],
        "A_C_imag": _A_in["C_imag"],
        "A_log_dt": _A_in["log_dt"],
        "A_D_skip": _A_in["D_skip"],
        "B_x": _B_in["x"],
        "B_w1": _B_in["w1"],
        "B_b1": _B_in["b1"],
        "B_w2": _B_in["w2"],
        "B_b2": _B_in["b2"],
    }

def reference(A_u, A_log_A_real, A_A_imag, A_C_real, A_C_imag, A_log_dt, A_D_skip, B_x, B_w1, B_b1, B_w2, B_b2):
    def _A_ref(u, log_A_real, A_imag, C_real, C_imag, log_dt, D_skip):
        L = u.shape[1]
        dt = torch.exp(log_dt)
        A = -torch.exp(log_A_real) + 1j * A_imag
        C = (C_real + 1j * C_imag) * dt[:, None]
        pos = (lambda x, dt: x.to(dt))(torch.arange(L), log_dt.dtype)
        P = torch.exp((A * dt[:, None])[:, :, None] * pos[None, None, :])
        K = 2.0 * torch.einsum("dn,dnl->dl", C, P).real
        L2 = 2 * L
        u_t = (lambda x, *d: x.permute(*d))(u, 0, 2, 1)
        Kf = (lambda x, *a, **k: torch.fft.rfft(x.float(), *a, **k))(K, L2)
        Uf = (lambda x, *a, **k: torch.fft.rfft(x.float(), *a, **k))(u_t, L2)
        y = (lambda x, *a, **k: torch.fft.irfft(x, *a, **k).to(torch.bfloat16))(Uf * Kf[None, :, :], L2)[:, :, :L]
        y = (lambda x, *d: x.permute(*d))(y, 0, 2, 1)
        return y + u * D_skip[None, None, :]

    def _B_ref(x, w1, b1, w2, b2):
        h = torch.einsum("bsd,df->bsf", x, w1) + b1
        zero = torch.zeros_like(h)
        r = torch.where(h > zero, h, zero)
        h = r * r
        out = torch.einsum("bsf,fd->bsd", h, w2) + b2
        return out

    x = A_u
    xn = _layernorm(x, axis=-1)
    s = _A_ref(xn, A_log_A_real, A_A_imag, A_C_real, A_C_imag, A_log_dt, A_D_skip)
    g = _silu(_B_ref(xn, B_w1, B_b1, B_w2, B_b2))
    return x + s * g
```

```cpp
#include <hip/hip_runtime.h>
#include <hip/hip_bf16.h>
#include <stdint.h>

#if defined(__HIP_DEVICE_COMPILE__)
#pragma clang attribute push (__attribute__((target("no-packed-fp32-ops"))), apply_to=function)
#endif
#define LAS __attribute__((address_space(3)))
typedef short v4s __attribute__((ext_vector_type(4)));
typedef short v8s __attribute__((ext_vector_type(8)));
typedef __bf16 v8b __attribute__((ext_vector_type(8)));
typedef __bf16 v2b __attribute__((ext_vector_type(2)));
typedef _Float16 v8h __attribute__((ext_vector_type(8)));
typedef _Float16 v2h __attribute__((ext_vector_type(2)));
typedef float v4f __attribute__((ext_vector_type(4)));
typedef float v2f __attribute__((ext_vector_type(2)));
typedef unsigned v4u __attribute__((ext_vector_type(4)));
typedef unsigned v2u __attribute__((ext_vector_type(2)));
typedef unsigned long long u64;

constexpr int SEQ = 2048, DM = 640, NM = 192, FF = 1536;

constexpr unsigned WS_STATS = 65536;
constexpr unsigned WS_XNT = 131072;
constexpr unsigned WS_W2P = 3u << 20;
constexpr unsigned WS_H = 5u << 20;
constexpr unsigned WS_S = 12u << 20;
constexpr unsigned WS_STAMP = 15u << 20;

constexpr int XB = 128 * 128, WROW = 192, WB = 64 * WROW, SBY = XB + WB;
constexpr int L_RING = 0;
constexpr int L_TAB = 4 * SBY;
constexpr int USTR = 4696, UPH = 1152;
constexpr int L_U = L_TAB + 3 * 8192;
constexpr int L_PACK = L_U + 3 * USTR + 8;
constexpr int L_CTL = L_PACK + 5120;
constexpr int L_END = L_CTL + 256;
static_assert(L_PACK % 16 == 0 && L_CTL % 16 == 0, "alignment");
static_assert(L_END <= 163840, "LDS budget");
constexpr int C_FULL1 = 0, C_FREE1 = 16, C_FULL2 = 32, C_FREE2 = 48, C_LN = 64, C_CS = 80, C_RED = 96, C_SS = 112;
constexpr int SPIN = 1 << 18;

struct Args {
  const unsigned short *x, *loga, *aim, *cre, *cim, *ldt, *dsk, *w1, *b1, *w2, *b2;
  unsigned short* out;
  char* ws;
};

__device__ __forceinline__ float u2f(unsigned v) { return __builtin_bit_cast(float, v); }
__device__ __forceinline__ float i2f(int v) { return __builtin_bit_cast(float, v); }
__device__ __forceinline__ unsigned f2u(float v) { return __builtin_bit_cast(unsigned, v); }
__device__ __forceinline__ int f2i(float v) { return __builtin_bit_cast(int, v); }
__device__ __forceinline__ void glds16(unsigned voff, const void* sbase, unsigned lds_dst) {
  asm volatile("s_mov_b32 m0, %2\n\ts_nop 0\n\tglobal_load_lds_dwordx4 %0, %1" :: "v"(voff), "s"(sbase), "s"(lds_dst) : "memory");
}
template <int N> __device__ __forceinline__ void vmwait() { asm volatile("s_waitcnt vmcnt(%0)" :: "n"(N) : "memory"); }
#define SB0() __builtin_amdgcn_sched_barrier(0)
__device__ __forceinline__ float bf2f(unsigned v) { return u2f(v << 16); }
__device__ __forceinline__ float bflo(unsigned v) { return u2f(v << 16); }
__device__ __forceinline__ float bfhi(unsigned v) { return u2f(v & 0xffff0000u); }
__device__ __forceinline__ unsigned pk_bf16(float a, float b) { v2f x = {a, b}; v2b r = __builtin_convertvector(x, v2b); return __builtin_bit_cast(unsigned, r); }
__device__ __forceinline__ unsigned pk_f16(float a, float b) { v2f x = {a, b}; v2h r = __builtin_convertvector(x, v2h); return __builtin_bit_cast(unsigned, r); }
__device__ __forceinline__ float rbf(float a) { return u2f(pk_bf16(a, 0.f) << 16); }
__device__ __forceinline__ unsigned rfl(unsigned v) { return __builtin_amdgcn_readfirstlane(v); }
template <int C> __device__ __forceinline__ float dppf(float v) { return i2f(__builtin_amdgcn_update_dpp(0, f2i(v), C, 0xf, 0xf, true)); }
__device__ __forceinline__ float rowsum16(float v) { v += dppf<0x128>(v); v += dppf<0x124>(v); v += dppf<0x122>(v); v += dppf<0x121>(v); return v; }
__device__ __forceinline__ float xor16(float v) { return i2f(__builtin_amdgcn_ds_swizzle(f2i(v), 0x401F)); }
__device__ __forceinline__ v2u trd(unsigned a) { v4s r = __builtin_amdgcn_ds_read_tr16_b64_v4i16((LAS v4s*)(size_t)a); return __builtin_bit_cast(v2u, r); }
__device__ __forceinline__ v4u tr2(unsigned a, unsigned b) { v2u lo = trd(a), hi = trd(b); return v4u{lo[0], lo[1], hi[0], hi[1]}; }
__device__ __forceinline__ v4u lds128(unsigned a) { return *(const LAS v4u*)(size_t)a; }
__device__ __forceinline__ v2u lds64(unsigned a) { return *(const LAS v2u*)(size_t)a; }
__device__ __forceinline__ void st128(unsigned a, v4u v) { *(LAS v4u*)(size_t)a = v; }
__device__ __forceinline__ void st64(unsigned a, v2u v) { *(LAS v2u*)(size_t)a = v; }
__device__ __forceinline__ void st32(unsigned a, unsigned v) { *(LAS unsigned*)(size_t)a = v; }
__device__ __forceinline__ void st16(unsigned a, unsigned v) { *(LAS unsigned short*)(size_t)a = (unsigned short)v; }
__device__ __forceinline__ v4f mfma_bf(v4u a, v4u b, v4f c) { return __builtin_amdgcn_mfma_f32_16x16x32_bf16(__builtin_bit_cast(v8b, a), __builtin_bit_cast(v8b, b), c, 0, 0, 0); }
__device__ __forceinline__ v4f mfma_h(v4u a, v4u b, v4f c) { return __builtin_amdgcn_mfma_f32_16x16x32_f16(__builtin_bit_cast(v8h, a), __builtin_bit_cast(v8h, b), c, 0, 0, 0); }

__device__ __forceinline__ void block_sync() {
  __builtin_amdgcn_fence(__ATOMIC_RELEASE, "workgroup");
  __builtin_amdgcn_s_barrier();
  __builtin_amdgcn_fence(__ATOMIC_ACQUIRE, "workgroup");
}
__device__ __forceinline__ void post(unsigned addr, unsigned val) {
  asm volatile("s_mov_b64 exec, 1\n\tds_write_b32 %0, %1\n\ts_mov_b64 exec, -1" :: "v"(addr), "v"(val) : "memory");
}
__device__ __forceinline__ v4u peek4(unsigned a) { return *(volatile LAS v4u*)(size_t)a; }
__device__ __forceinline__ v2u peek2(unsigned a) { return *(volatile LAS v2u*)(size_t)a; }
__device__ __forceinline__ unsigned peek1(unsigned a) { return *(volatile LAS unsigned*)(size_t)a; }
__device__ __forceinline__ unsigned min4(v4u f) { return rfl(min(min(f[0], f[1]), min(f[2], f[3]))); }
__device__ __forceinline__ void wait4(unsigned a, unsigned tgt) { for (int i = 0; i < SPIN; ++i) if (min4(peek4(a)) >= tgt) break; }
__device__ __forceinline__ void wait2(unsigned a, unsigned tgt) { for (int i = 0; i < SPIN; ++i) { v2u f = peek2(a); if (rfl(min(f[0], f[1])) >= tgt) break; } }
__device__ __forceinline__ void wait1(unsigned a, unsigned tgt) { for (int i = 0; i < SPIN; ++i) if (rfl(peek1(a)) >= tgt) break; }

#define KN() do {} while (0)
struct cf { float r, i; };
__device__ __forceinline__ cf cmul(cf a, cf b) { return cf{a.r * b.r - a.i * b.i, a.r * b.i + a.i * b.r}; }
__device__ __forceinline__ cf cexpp(float a2, float brev, float p) {
  const float m = __builtin_amdgcn_exp2f(a2 * p), f = __builtin_amdgcn_fractf(brev * p);
  return cf{m * __builtin_amdgcn_cosf(f), m * __builtin_amdgcn_sinf(f)};
}

#define STAMP(k) do {} while (0)

__device__ __forceinline__ void ln_compute(const Args& A, int u, int lw, int lane, unsigned lds) {
  const int tk = 2 * lw + (lane >> 5), i = lane & 31;
  const char* row = (const char*)A.x + (size_t)(8 * u + tk) * (DM * 2) + i * 8;
  v2u d[5];
#pragma unroll
  for (int c = 0; c < 5; ++c) d[c] = *(const v2u*)(row + c * 256);
  float v[20];
#pragma unroll
  for (int c = 0; c < 5; ++c) { v[4 * c] = bflo(d[c][0]); v[4 * c + 1] = bfhi(d[c][0]); v[4 * c + 2] = bflo(d[c][1]); v[4 * c + 3] = bfhi(d[c][1]); }
  float s = 0;
#pragma unroll
  for (int e = 0; e < 20; ++e) s += v[e];
  s = rowsum16(s); s += xor16(s);
  const float mean = s / (float)DM;
  float q = 0;
#pragma unroll
  for (int e = 0; e < 20; ++e) { const float t = v[e] - mean; q += t * t; }
  q = rowsum16(q); q += xor16(q);
  const float m = rbf(mean), sd = rbf(sqrtf(rbf(rbf(q / (float)DM) + 1e-5f))), rs = 1.0f / sd;
  const unsigned dst = lds + L_U + tk * 1280 + i * 8;
#pragma unroll
  for (int c = 0; c < 5; ++c) {
    float o[4];
#pragma unroll
    for (int e = 0; e < 4; ++e) o[e] = rbf(rbf(v[4 * c + e] - m) * rs);
    st64(dst + c * 256, v2u{pk_f16(o[0], o[1]), pk_f16(o[2], o[3])});
  }
  if (i == 0) {
    const unsigned ms = (f2u(m) >> 16) | (f2u(sd) & 0xffff0000u);
    *(v4u*)(A.ws + WS_STATS + (8 * u + tk) * 16) = v4u{ms, 0, ms, 0};
  }
  post(lds + L_CTL + C_LN + lw * 4, 1);
}

__device__ __forceinline__ void ln_publish(const Args& A, int u, int lane, unsigned lds) {
  wait4(lds + L_CTL + C_LN, 1);
  const int q = (lane & 15) >> 2, p = lane & 3, g = lane >> 4;
  const unsigned a0 = lds + L_U + ((q >> 1) + 4 * (q & 1)) * 1280 + g * 32 + p * 8;
  char* dst = A.ws + WS_XNT + (size_t)u * 10240 + lane * 16;
#pragma unroll
  for (int i = 0; i < 10; ++i) *(v4u*)(dst + i * 1024) = tr2(a0 + i * 128, a0 + 2 * 1280 + i * 128);
}

__device__ __forceinline__ void pack_unit(const Args& A, int unit, int lane, unsigned lds) {
  const int s = unit >> 3, nt = unit & 7;
#pragma unroll
  for (int i = 0; i < 5; ++i) {
    const int ci = i * 64 + lane, row = ci / 10, c = ci % 10;
    glds16((unsigned)((32 * s + row) * (DM * 2) + nt * 160 + c * 16), A.w2, lds + L_PACK + i * 1024);
  }
  vmwait<0>();
  const int q = (lane & 15) >> 2, p = lane & 3, g = lane >> 4;
  const unsigned a0 = lds + L_PACK + (4 * g + q) * 160 + p * 8;
  char* dst = A.ws + WS_W2P + (size_t)(nt * 48 + s) * 5120 + lane * 16;
  v4u f[5];
#pragma unroll
  for (int ct = 0; ct < 5; ++ct) f[ct] = tr2(a0 + ct * 32, a0 + 16 * 160 + ct * 32);
#pragma unroll
  for (int ct = 0; ct < 5; ++ct) *(v4u*)(dst + ct * 1024) = f[ct];
  vmwait<0>();
}

__device__ __forceinline__ void g1_load(const Args& A, int mt, int nt, int lw, int lane, unsigned lds) {
  unsigned voff[7], loff[7];
#pragma unroll
  for (int i = 0; i < 4; ++i) {
    const int row = 32 * lw + 8 * i + (lane >> 3), c = (lane & 7) ^ ((row >> 1) & 7);
    voff[i] = (unsigned)((mt * 128 + row) * (DM * 2) + c * 16);
    loff[i] = (4 * lw + i) * 1024;
  }
#pragma unroll
  for (int i = 0; i < 3; ++i) {
    const int ci = (3 * lw + i) * 64 + lane, row = ci / 12, p = ci % 12, sw = ((row >> 3) & 1) << 1;
    voff[4 + i] = (unsigned)(row * (FF * 2) + nt * WROW + (p ^ sw) * 16);
    loff[4 + i] = XB + (3 * lw + i) * 1024;
  }
  auto issue = [&](int s) {
    const char* xb = (const char*)A.x + s * 128;
    const char* wb = (const char*)A.w1 + (size_t)s * (64 * FF * 2);
    const unsigned slot = lds + L_RING + (s & 3) * SBY;
#pragma unroll
    for (int i = 0; i < 7; ++i) glds16(voff[i], i < 4 ? xb : wb, slot + loff[i]);
  };
  const unsigned full = lds + L_CTL + C_FULL1 + lw * 4, freew = lds + L_CTL + C_FREE1;
#pragma unroll
  for (int s = 0; s < 4; ++s) issue(s);
  vmwait<21>(); post(full, 1);
#pragma unroll 1
  for (int s = 1; s < 7; ++s) {
    vmwait<14>(); post(full, s + 1);
    wait4(freew, s);
    issue(s + 3);
  }
  vmwait<14>(); post(full, 8);
  vmwait<7>(); post(full, 9);
  vmwait<0>(); post(full, 10);
}

struct Frag { v4u a[6]; v4u b[2]; };

__device__ __forceinline__ void g1_compute(const Args& A, int mt, int nt, int w, int lane, unsigned lds) {
  const int t16 = lane & 15, g = lane >> 4;
  unsigned xa[2], wa[2];
  {
    const int row = 32 * w + t16, sw = (row >> 1) & 7;
    xa[0] = lds + row * 128 + ((g ^ sw) * 16); xa[1] = lds + row * 128 + (((4 + g) ^ sw) * 16);
    const int q = t16 >> 2, p = lane & 3, r = 8 * g + q, o = g & 1;
    wa[0] = lds + XB + r * WROW + ((2 * o + (p >> 1)) * 16) + 8 * (p & 1);
    wa[1] = lds + XB + r * WROW + ((2 * (1 - o) + (p >> 1)) * 16) + 8 * (p & 1);
  }
  v4f acc[6][2], cs[3];
#pragma unroll
  for (int a = 0; a < 6; ++a) { acc[a][0] = v4f{0, 0, 0, 0}; acc[a][1] = v4f{0, 0, 0, 0}; }
#pragma unroll
  for (int a = 0; a < 3; ++a) cs[a] = v4f{0, 0, 0, 0};
  const v4u ones = {0x3f803f80u, 0x3f803f80u, 0x3f803f80u, 0x3f803f80u};
  const unsigned full = lds + L_CTL + C_FULL1, freew = lds + L_CTL + C_FREE1 + w * 4;
  auto rdB = [&](Frag& f, unsigned so, int kk, int tt) { f.b[tt] = lds128(xa[kk] + so + tt * 2048); };
  auto rdA = [&](Frag& f, unsigned so, int kk, int ft) {
    const unsigned p = wa[ft & 1] + so + (ft >> 1) * 64 + kk * 32 * WROW;
    f.a[ft] = tr2(p, p + 4 * WROW);
  };
  auto group = [&](Frag& cur, Frag& nxt, unsigned so, int kk) {
#pragma unroll
    for (int m = 0; m < 12; ++m) {
      const int ft = m >> 1, tt = m & 1;
      acc[ft][tt] = mfma_bf(cur.a[ft], cur.b[tt], acc[ft][tt]);
      if (m == 0) { rdB(nxt, so, kk, 0); rdB(nxt, so, kk, 1); }
      else if (m <= 6) rdA(nxt, so, kk, m - 1);
      SB0();
    }
  };
  auto colsum = [&](Frag& cur) {
    if (w & 1) {
#pragma unroll
      for (int a = 0; a < 3; ++a) cs[a] = mfma_bf(cur.a[3 + a], ones, cs[a]);
    } else {
#pragma unroll
      for (int a = 0; a < 3; ++a) cs[a] = mfma_bf(cur.a[a], ones, cs[a]);
    }
    SB0();
  };
  Frag fa, fb;
  wait4(full, 1);
  rdB(fa, 0, 0, 0); rdB(fa, 0, 0, 1);
#pragma unroll
  for (int ft = 0; ft < 6; ++ft) rdA(fa, 0, 0, ft);
  SB0();
  unsigned so = 0;
#pragma unroll 1
  for (int s = 0; s < 10; ++s) {
    group(fa, fb, so, 1);
    post(freew, s + 1);
    const v4u fw = peek4(full);
    if (w < 2) colsum(fa);
    const unsigned need = s + 2 > 10 ? 10 : s + 2;
    if (min4(fw) < need) wait4(full, need);
    so = (s & 3) == 3 ? 0 : so + SBY;
    SB0();
    group(fb, fa, so, 0);
    if (w >= 2) colsum(fb);
  }
  const unsigned csl = lds + L_RING;
  wait4(lds + L_CTL + C_FREE1, 10);
  if (t16 == 0) {
#pragma unroll
    for (int a = 0; a < 3; ++a) st128(csl + ((w >> 1) * 96 + (3 * (w & 1) + a) * 16 + 4 * g) * 4, __builtin_bit_cast(v4u, cs[a]));
  }
  post(lds + L_CTL + C_CS + w * 4, 1);
  v2u bb[6];
#pragma unroll
  for (int ft = 0; ft < 6; ++ft) bb[ft] = *(const v2u*)(A.b1 + nt * 96 + ft * 16 + 4 * g);
  float mean[2], rstd[2];
#pragma unroll
  for (int tt = 0; tt < 2; ++tt) {
    const unsigned ms = *(const unsigned*)(A.ws + WS_STATS + (mt * 128 + 32 * w + 16 * tt + t16) * 16);
    mean[tt] = bflo(ms); rstd[tt] = 1.0f / bfhi(ms);
  }
  wait4(lds + L_CTL + C_CS, 1);
#pragma unroll
  for (int sp = 0; sp < 3; ++sp) {
    unsigned pk[2][4];
#pragma unroll
    for (int hf = 0; hf < 2; ++hf) {
      const int ft = 2 * sp + hf;
      const v4f c4 = __builtin_bit_cast(v4f, lds128(csl + (ft * 16 + 4 * g) * 4)) + __builtin_bit_cast(v4f, lds128(csl + (96 + ft * 16 + 4 * g) * 4));
      const float bv[4] = {bflo(bb[ft][0]), bfhi(bb[ft][0]), bflo(bb[ft][1]), bfhi(bb[ft][1])};
#pragma unroll
      for (int tt = 0; tt < 2; ++tt) {
        float o[4];
#pragma unroll
        for (int r = 0; r < 4; ++r) {
          float v = rstd[tt] * (acc[ft][tt][r] - mean[tt] * c4[r]);
          v = fmaxf(rbf(rbf(v) + bv[r]), 0.f);
          o[r] = v * v;
        }
        pk[tt][2 * hf] = pk_bf16(o[0], o[1]); pk[tt][2 * hf + 1] = pk_bf16(o[2], o[3]);
      }
    }
#pragma unroll
    for (int tt = 0; tt < 2; ++tt) {
      const int T = mt * 8 + 2 * w + tt, S = nt * 3 + sp;
      *(v4u*)(A.ws + WS_H + (size_t)(T * 48 + S) * 1024 + lane * 16) = v4u{pk[tt][0], pk[tt][1], pk[tt][2], pk[tt][3]};
    }
  }
}

__device__ __forceinline__ void conv_channel(const Args& A, int ch, int c, int lane, unsigned lds) {
  const int t16 = lane & 15, g = lane >> 4, q4 = t16 >> 2, p4 = lane & 3;
  const unsigned tab = lds + L_TAB + c * 8192, ub = lds + L_U + c * USTR;
  unsigned short la[3], ai[3], cr[3], ci[3];
#pragma unroll
  for (int p = 0; p < 3; ++p) {
    const int n = ch * NM + p * 64 + lane;
    la[p] = A.loga[n]; ai[p] = A.aim[n]; cr[p] = A.cre[n]; ci[p] = A.cim[n];
  }
  const float dt = rbf(expf(bf2f(A.ldt[ch]))), dsk = bf2f(A.dsk[ch]);
  float bound = 0;
#pragma unroll
  for (int p = 0; p < 3; ++p) bound += fabsf(bf2f(cr[p])) + fabsf(bf2f(ci[p]));
  bound = rowsum16(bound);
  bound = i2f(__builtin_amdgcn_readlane(f2i(bound), 0)) + i2f(__builtin_amdgcn_readlane(f2i(bound), 16)) +
          i2f(__builtin_amdgcn_readlane(f2i(bound), 32)) + i2f(__builtin_amdgcn_readlane(f2i(bound), 48));
  bound = 2.0f * bound * dt + fabsf(dsk);
  int ex = (int)((f2u(bound) >> 23) & 0xff) - 126;
  ex = ex < -100 ? -100 : (ex > 100 ? 100 : ex);
  const float sigma = u2f((unsigned)(127 + 13 - ex) << 23), isigma = u2f((unsigned)(127 - 13 + ex) << 23);

  v4f kt[3];
#pragma unroll
  for (int a = 0; a < 3; ++a) kt[a] = v4f{0, 0, 0, 0};
  const unsigned wre = tab + lane * 32, wim = wre + 2048;
  const unsigned rdb = tab + (g >> 1) * 2048 + (4 * (g & 1) + q4) * 32 + p4 * 8;
  auto put = [&](unsigned base, const cf* v, float sim) {
    st128(base, v4u{pk_f16(v[0].r, v[1].r), pk_f16(v[2].r, v[3].r), pk_f16(v[4].r, v[5].r), pk_f16(v[6].r, v[7].r)});
    st128(base + 16, v4u{pk_f16(v[8].r, v[9].r), pk_f16(v[10].r, v[11].r), pk_f16(v[12].r, v[13].r), pk_f16(v[14].r, v[15].r)});
    st128(base + 2048, v4u{pk_f16(sim * v[0].i, sim * v[1].i), pk_f16(sim * v[2].i, sim * v[3].i), pk_f16(sim * v[4].i, sim * v[5].i), pk_f16(sim * v[6].i, sim * v[7].i)});
    st128(base + 2064, v4u{pk_f16(sim * v[8].i, sim * v[9].i), pk_f16(sim * v[10].i, sim * v[11].i), pk_f16(sim * v[12].i, sim * v[13].i), pk_f16(sim * v[14].i, sim * v[15].i)});
  };
  (void)wim;
#pragma unroll 1
  for (int p = 0; p < 3; ++p) {
    const unsigned short lap = p == 0 ? la[0] : (p == 1 ? la[1] : la[2]), aip = p == 0 ? ai[0] : (p == 1 ? ai[1] : ai[2]);
    const unsigned short crp = p == 0 ? cr[0] : (p == 1 ? cr[1] : cr[2]), cip = p == 0 ? ci[0] : (p == 1 ? ci[1] : ci[2]);
    const float a2 = -rbf(expf(bf2f(lap))) * dt * 1.44269504088896341f, brev = bf2f(aip) * dt * 0.15915494309189535f;
    const float cs = 2.0f * sigma * dt;
    const cf C0 = {bf2f(crp) * cs, bf2f(cip) * cs};
    const cf w1 = cexpp(a2, brev, 1.f), w8 = cexpp(a2, brev, 8.f), G = cexpp(a2, brev, 128.f);
    cf v[16];
    v[0] = cf{1.f, 0.f}; v[1] = w8;
#pragma unroll
    for (int j = 2; j < 16; ++j) v[j] = cmul(v[j - 1], w8);
    put(wre + 4096, v, -1.f);
    v4u yf[4];
#pragma unroll
    for (int m = 0; m < 4; ++m) yf[m] = tr2(rdb + 4096 + m * 512, rdb + 4096 + m * 512 + 256);
    cf e[8];
    e[0] = cf{1.f, 0.f}; e[1] = w1;
#pragma unroll
    for (int k = 2; k < 8; ++k) e[k] = cmul(e[k - 1], w1);
    cf H = C0;
#pragma unroll
    for (int h = 0; h < 2; ++h) {
      v[8 * h] = H;
#pragma unroll
      for (int k = 1; k < 8; ++k) v[8 * h + k] = cmul(H, e[k]);
      H = cmul(H, G);
    }
    put(wre, v, 1.f);
#pragma unroll
    for (int m = 0; m < 4; ++m) kt[0] = mfma_h(tr2(rdb + m * 512, rdb + m * 512 + 256), yf[m], kt[0]);
    KN();
#pragma unroll
    for (int h = 0; h < 2; ++h) {
      v[4 * h] = H; v[4 * h + 1] = cmul(H, e[2]); v[4 * h + 2] = cmul(H, e[4]); v[4 * h + 3] = cmul(H, e[6]);
      H = cmul(H, G);
    }
#pragma unroll
    for (int h = 0; h < 4; ++h) { v[8 + 2 * h] = H; v[9 + 2 * h] = cmul(H, e[4]); H = cmul(H, G); }
    put(wre, v, 1.f);
#pragma unroll
    for (int m = 0; m < 4; ++m) kt[1] = mfma_h(tr2(rdb + m * 512, rdb + m * 512 + 256), yf[m], kt[1]);
    KN();
#pragma unroll
    for (int i = 0; i < 9; ++i) { v[i] = H; if (i < 8) H = cmul(H, G); }
#pragma unroll
    for (int i = 9; i < 16; ++i) v[i] = cf{0.f, 0.f};
    put(wre, v, 1.f);
#pragma unroll
    for (int m = 0; m < 4; ++m) kt[2] = mfma_h(tr2(rdb + m * 512, rdb + m * 512 + 256), yf[m], kt[2]);
    KN();
  }
  const unsigned kb = tab + 32;
  if (lane < 4) st64(tab + lane * 8, v2u{0, 0});
  {
    if (lane == 0) kt[0][0] += dsk * sigma;
    const int P = 4 * (g & 1) + 128 * (g >> 1) + 8 * t16;
    st64(kb + 2 * P, v2u{pk_f16(kt[0][0], kt[0][1]), pk_f16(kt[0][2], kt[0][3])});
  }
  if (g < 2) {
    const int P0 = 256 + 128 * g + 8 * t16;
    const unsigned a01 = pk_f16(kt[1][0], kt[1][1]), a23 = pk_f16(kt[1][2], kt[1][3]);
    const unsigned v0 = a01 & 0xffffu, v1 = a01 >> 16, v2 = a23 & 0xffffu, v3 = a23 >> 16;
    st128(kb + 2 * P0, v4u{v0 | (v0 << 16), v1 | (v2 << 16), v2 | (v2 << 16), v3 | (v3 << 16)});
  } else {
#pragma unroll
    for (int h = 0; h < 2; ++h) {
      const int W = 512 + 256 * (g - 2) + 128 * h + 8 * t16;
      const unsigned ab = pk_f16(kt[1][2 * h], kt[1][2 * h + 1]), a = ab & 0xffffu, b = ab >> 16;
      st128(kb + 2 * W, v4u{a | (a << 16), a | (b << 16), b | (b << 16), b | (b << 16)});
    }
  }
  unsigned k2[4];
  {
    const unsigned a = pk_f16(kt[2][0], kt[2][1]), b = pk_f16(kt[2][2], kt[2][3]);
    k2[0] = (a & 0xffffu) * 0x10001u; k2[1] = (a >> 16) * 0x10001u; k2[2] = (b & 0xffffu) * 0x10001u; k2[3] = (b >> 16) * 0x10001u;
  }
  if (g < 2) {
#pragma unroll
    for (int e = 0; e < 4; ++e) {
      const int P = 1024 + 128 * (4 * g + e) + 8 * t16;
      if (P != 1024) st64(kb + 2 * (P - 4), v2u{k2[e], k2[e]});
      st64(kb + 2 * P, v2u{k2[e], k2[e]});
    }
  } else if (lane == 32) st64(kb + 2 * 2044, v2u{k2[0], k2[0]});
  if (g < 2) {
    const int P0 = 256 + 128 * g + 8 * t16;
    if (P0 != 256) st16(kb + 2 * (P0 - 1), pk_f16(kt[1][0], 0.f));
#pragma unroll
    for (int e = 0; e < 4; ++e) {
      const int P = 1024 + 128 * (4 * g + e) + 8 * t16;
      if (!(t16 & 1)) st16(kb + 2 * (P + 4), k2[e]);
    }
    if (lane == 0) st32(kb + 2 * 1022, k2[0]);
  } else {
#pragma unroll
    for (int h = 0; h < 2; ++h) {
      const int W = 512 + 256 * (g - 2) + 128 * h + 8 * t16;
      const unsigned a = (pk_f16(kt[1][2 * h], 0.f) & 0xffffu) * 0x10001u;
      if (W != 512) st32(kb + 2 * (W - 2), a); else st16(kb + 2 * 511, a);
    }
  }
  v4u T[16];
  {
    const unsigned ta = kb + 2 * (112 - 4 * (8 * g + q4) + 4 * p4);
#pragma unroll
    for (int s = 0; s < 16; ++s) T[s] = tr2(ta + 256 * s, ta + 256 * s - 32);
  }
  if (lane < 32) {
#pragma unroll
    for (int r = 0; r < 4; ++r) st32(ub + r * UPH + lane * 4, 0);
  }
  {
    const char* src = A.ws + WS_XNT + (size_t)ch * 16;
    v4u pc[4];
#pragma unroll
    for (int i = 0; i < 4; ++i) pc[i] = *(const v4u*)(src + (size_t)(i * 64 + lane) * 10240);
#pragma unroll
    for (int i = 0; i < 4; ++i)
#pragma unroll
      for (int r = 0; r < 4; ++r) st32(ub + r * UPH + 128 + (i * 64 + lane) * 4, pc[i][r]);
  }
  v4f acc[8];
#pragma unroll
  for (int C = 0; C < 8; ++C) acc[C] = v4f{0, 0, 0, 0};
  const int ord = 4 * t16 + g, po = (ord + 63) & 63;
  const int psrc = (16 * (po & 3) + (po >> 2)) * 4;
#pragma unroll 1
  for (int r = 3; r >= 0; --r) {
    if (r != 3) {
      float t[8];
#pragma unroll
      for (int C = 0; C < 8; ++C) t[C] = i2f(__builtin_amdgcn_ds_bpermute(psrc, f2i(acc[C][3])));
#pragma unroll
      for (int C = 7; C >= 0; --C) {
        const float first = lane == 0 ? (C == 0 ? 0.f : t[C > 0 ? C - 1 : 0]) : t[C];
        acc[C] = v4f{first, acc[C][0], acc[C][1], acc[C][2]};
      }
    }
    const unsigned ua = ub + r * UPH + 2 * (64 - 28 + 4 * t16 + 8 * g);
#pragma unroll
    for (int q = -1; q <= 14; ++q) {
      const v2u lo = lds64(ua + 64 * q), hi = lds64(ua + 64 * q + 8);
      const v4u B = {lo[0], lo[1], hi[0], hi[1]};
#pragma unroll
      for (int C = (q + 1) / 2; C < 8; ++C) acc[C] = mfma_h(T[2 * C - q], B, acc[C]);
    }
  }
#pragma unroll
  for (int C = 0; C < 8; ++C) {
    const v4f y = acc[C] * isigma;
    *(v2u*)(A.ws + WS_S + ((size_t)(4 * C + (t16 >> 2)) * DM + ch) * 128 + (16 * (t16 & 3) + 4 * g) * 2) = v2u{pk_bf16(y[0], y[1]), pk_bf16(y[2], y[3])};
  }
}

__device__ __forceinline__ void g2_load(const Args& A, int nt2, int rot, int lw, int lane, unsigned lds) {
  const int kh = lw & 1, par = lw >> 1;
  const char* base = A.ws + WS_W2P + (size_t)nt2 * 48 * 5120;
  const unsigned voff = lane * 16;
  auto issue = [&](int n) {
    const int i = 2 * n + par;
    int s = i + rot; s = s >= 24 ? s - 24 : s;
    const char* src = base + (size_t)(24 * kh + s) * 5120;
    const unsigned slot = lds + L_RING + (kh * 10 + (i >= 20 ? i - 20 : (i >= 10 ? i - 10 : i))) * 5120;
#pragma unroll
    for (int c = 0; c < 5; ++c) glds16(voff, src + c * 1024, slot + c * 1024);
  };
  const unsigned full = lds + L_CTL + C_FULL2 + (kh * 2 + par) * 4, freew = lds + L_CTL + C_FREE2 + kh * 8;
#pragma unroll 1
  for (int n = 0; n < 5; ++n) issue(n);
  vmwait<20>(); post(full, 1);
#pragma unroll 1
  for (int n = 1; n < 8; ++n) {
    vmwait<15>(); post(full, n + 1);
    wait2(freew, 2 * (n - 1) + par + 1);
    issue(n + 4);
  }
  vmwait<15>(); post(full, 9);
  vmwait<10>(); post(full, 10);
  vmwait<5>(); post(full, 11);
  vmwait<0>(); post(full, 12);
}

__device__ __forceinline__ void s_stage(const Args& A, int mt2, int nt2, int lane, unsigned lds) {
  const char* src = A.ws + WS_S + ((size_t)mt2 * DM + 80 * nt2) * 128;
#pragma unroll
  for (int i = 0; i < 10; ++i) glds16(lane * 16, src + i * 1024, lds + L_TAB + i * 1024);
  vmwait<0>();
  post(lds + L_CTL + C_SS, 1);
}

__device__ __forceinline__ void gldr(v4u& d, unsigned voff, const void* sb) {
  asm volatile("global_load_dwordx4 %0, %1, %2" : "+v"(d) : "v"(voff), "s"(sb) : "memory");
}

__device__ __forceinline__ void g2_compute(const Args& A, int mt2, int nt2, int rot, int w, int lane, unsigned lds) {
  const int th = w & 1, kh = w >> 1, t16 = lane & 15, g = lane >> 4;
  const int T0 = mt2 * 4 + 2 * th + kh, T1 = mt2 * 4 + 2 * th + (1 - kh);
  const char* h0 = A.ws + WS_H + (size_t)(T0 * 48 + 24 * kh) * 1024;
  const char* h1 = A.ws + WS_H + (size_t)(T1 * 48 + 24 * kh) * 1024;
  const unsigned voff = lane * 16;
  v4u hb[6][2], wf[2][5];
  v4f acc[2][5];
#pragma unroll
  for (int a = 0; a < 5; ++a) { acc[0][a] = v4f{0, 0, 0, 0}; acc[1][a] = v4f{0, 0, 0, 0}; }
  int sl = rot;
  auto hload = [&](int k) {
    gldr(hb[k][0], voff, h0 + sl * 1024); gldr(hb[k][1], voff, h1 + sl * 1024);
    sl = sl == 23 ? 0 : sl + 1;
  };
#pragma unroll
  for (int k = 0; k < 6; ++k) { hb[k][0] = v4u{0, 0, 0, 0}; hb[k][1] = v4u{0, 0, 0, 0}; hload(k); }
  const unsigned full = lds + L_CTL + C_FULL2 + kh * 8, freew = lds + L_CTL + C_FREE2 + (kh * 2 + th) * 4;
  const unsigned wbase = lds + L_RING + kh * 10 * 5120 + lane * 16;
  wait1(full, 1);
#pragma unroll
  for (int c = 0; c < 5; ++c) wf[0][c] = lds128(wbase + c * 1024);
  post(freew, 1);
  unsigned slot = 1;
#pragma unroll 1
  for (int it = 0; it < 4; ++it) {
#pragma unroll
    for (int k = 0; k < 6; ++k) {
      const int i = it * 6 + k;
      const unsigned need = (i + 1 > 23 ? 23 : i + 1) / 2 + 1;
      const unsigned fw = peek1(full + ((k + 1) & 1) * 4);
      if (it < 3) vmwait<10>(); else if (k == 0) vmwait<10>(); else if (k == 1) vmwait<8>(); else if (k == 2) vmwait<6>(); else if (k == 3) vmwait<4>(); else if (k == 4) vmwait<2>(); else vmwait<0>();
      asm volatile("" : "+v"(hb[k][0]), "+v"(hb[k][1]));
      SB0();
      acc[0][0] = mfma_bf(wf[k & 1][0], hb[k][0], acc[0][0]); SB0();
      acc[1][0] = mfma_bf(wf[k & 1][0], hb[k][1], acc[1][0]); SB0();
      if (rfl(fw) < need) wait1(full + ((k + 1) & 1) * 4, need);
      const unsigned sa = wbase + slot * 5120;
#pragma unroll
      for (int c = 1; c < 5; ++c) {
        acc[0][c] = mfma_bf(wf[k & 1][c], hb[k][0], acc[0][c]);
        if (c == 1) { wf[(k + 1) & 1][0] = lds128(sa); wf[(k + 1) & 1][1] = lds128(sa + 1024); }
        if (c == 2) { wf[(k + 1) & 1][2] = lds128(sa + 2048); wf[(k + 1) & 1][3] = lds128(sa + 3072); }
        if (c == 3) { wf[(k + 1) & 1][4] = lds128(sa + 4096); }
        SB0();
        acc[1][c] = mfma_bf(wf[k & 1][c], hb[k][1], acc[1][c]); SB0();
      }
      post(freew, i + 2);
      slot = slot == 9 ? 0 : slot + 1;
      if (it < 3) hload(k);
      SB0();
    }
  }
  wait4(lds + L_CTL + C_FREE2, 25);
  const unsigned ex = lds + L_RING;
#pragma unroll
  for (int c = 0; c < 5; ++c) st128(ex + (w * 5 + c) * 1024 + lane * 16, __builtin_bit_cast(v4u, acc[1][c]));
  post(lds + L_CTL + C_RED + w * 4, 1);
  const int tok = T0 * 16 + t16;
  v2u xv[5], bv[5];
#pragma unroll
  for (int c = 0; c < 5; ++c) {
    xv[c] = *(const v2u*)(A.x + (size_t)tok * DM + 80 * nt2 + 16 * c + 4 * g);
    bv[c] = *(const v2u*)(A.b2 + 80 * nt2 + 16 * c + 4 * g);
  }
  wait1(lds + L_CTL + C_RED + (w ^ 2) * 4, 1);
  wait1(lds + L_CTL + C_SS, 1);
  const int q4 = t16 >> 2, p4 = lane & 3;
  const unsigned sa = lds + L_TAB + (4 * g + q4) * 128 + ((T0 & 3) * 16 + 4 * p4) * 2;
#pragma unroll
  for (int c = 0; c < 5; ++c) {
    const v4f o = acc[0][c] + __builtin_bit_cast(v4f, lds128(ex + ((w ^ 2) * 5 + c) * 1024 + lane * 16));
    const v2u sv = trd(sa + c * 16 * 128);
    const float b4[4] = {bflo(bv[c][0]), bfhi(bv[c][0]), bflo(bv[c][1]), bfhi(bv[c][1])};
    const float s4[4] = {bflo(sv[0]), bfhi(sv[0]), bflo(sv[1]), bfhi(sv[1])};
    const float x4[4] = {bflo(xv[c][0]), bfhi(xv[c][0]), bflo(xv[c][1]), bfhi(xv[c][1])};
    float r[4];
#pragma unroll
    for (int e = 0; e < 4; ++e) {
      const float v = rbf(rbf(o[e]) + b4[e]);
      const float gt = rbf(v / (1.0f + __expf(-v)));
      r[e] = x4[e] + rbf(s4[e] * gt);
    }
    *(v2u*)(A.out + (size_t)tok * DM + 80 * nt2 + 16 * c + 4 * g) = v2u{pk_bf16(r[0], r[1]), pk_bf16(r[2], r[3])};
  }
}

template <int PH>
__global__ __launch_bounds__(768, 3) void s4d_mlp(Args A) {
  extern __shared__ __attribute__((aligned(16))) char smem[];
  const unsigned lds = (unsigned)(size_t)(LAS char*)smem;
  const int lane = threadIdx.x & 63, wv = __builtin_amdgcn_readfirstlane(threadIdx.x >> 6);
  const int b = blockIdx.x, xcd = b & 7, j = b >> 3;
  if (threadIdx.x < 64) *(LAS unsigned*)(size_t)(lds + L_CTL + threadIdx.x * 4) = 0;
  block_sync();
  STAMP(0);
  const int mt = 4 * (xcd >> 1) + (j >> 3), nt = 8 * (xcd & 1) + (j & 7);
  const int mt2 = 4 * xcd + (j >> 3), nt2 = j & 7, rot = (3 * (j >> 3) + 12 * (xcd & 1)) % 24;
  if (wv < 4) {

    if (PH & 2) g1_compute(A, mt, nt, wv, lane, lds);
    if (PH & 4) g2_compute(A, mt2, nt2, rot, wv, lane, lds);
  } else if (wv < 8) {
    if (PH & 1) ln_compute(A, 16 * mt + nt, wv - 4, lane, lds);

    if (PH & 2) g1_load(A, mt, nt, wv - 4, lane, lds);
    if (PH & 4) g2_load(A, nt2, rot, wv - 4, lane, lds);
  } else if (wv < 11) {
    if (PH & 2) {
      const int c = wv - 8;
      if (c < 3 - (b & 1)) conv_channel(A, 5 * (b >> 1) + 3 * (b & 1) + c, c, lane, lds);
    }
  } else {
    if (PH & 1) {
      pack_unit(A, b, lane, lds);
      if (b < 128) pack_unit(A, 256 + b, lane, lds);
      ln_publish(A, 16 * mt + nt, lane, lds);
    }
    if (PH & 4) s_stage(A, mt2, nt2, lane, lds);
  }
  STAMP(1);
}

#if defined(__HIP_DEVICE_COMPILE__)
#pragma clang attribute pop
#endif

extern "C" void kernel_launch(void* const* d_in, const int* in_sizes, int n_in, void* d_out, int out_size, void* d_ws, size_t ws_size, hipStream_t stream) {
  Args A;
  A.x = (const unsigned short*)d_in[0]; A.loga = (const unsigned short*)d_in[1]; A.aim = (const unsigned short*)d_in[2];
  A.cre = (const unsigned short*)d_in[3]; A.cim = (const unsigned short*)d_in[4]; A.ldt = (const unsigned short*)d_in[5];
  A.dsk = (const unsigned short*)d_in[6]; A.w1 = (const unsigned short*)d_in[8]; A.b1 = (const unsigned short*)d_in[9];
  A.w2 = (const unsigned short*)d_in[10]; A.b2 = (const unsigned short*)d_in[11];
  A.out = (unsigned short*)d_out; A.ws = (char*)d_ws;
  hipFuncSetAttribute((const void*)s4d_mlp<1>, hipFuncAttributeMaxDynamicSharedMemorySize, L_END);
  hipFuncSetAttribute((const void*)s4d_mlp<2>, hipFuncAttributeMaxDynamicSharedMemorySize, L_END);
  hipFuncSetAttribute((const void*)s4d_mlp<4>, hipFuncAttributeMaxDynamicSharedMemorySize, L_END);
  s4d_mlp<1><<<256, 768, L_END, stream>>>(A);
  s4d_mlp<2><<<256, 768, L_END, stream>>>(A);
  s4d_mlp<4><<<256, 768, L_END, stream>>>(A);
}
```

```cpp
#include <hip/hip_runtime.h>
#include <hip/hip_bf16.h>
#include <stdint.h>
#include <utility>

#if defined(__HIP_DEVICE_COMPILE__)
#pragma clang attribute push (__attribute__((target("no-packed-fp32-ops"))), apply_to=function)
#endif
#define LAS __attribute__((address_space(3)))
typedef short v4s __attribute__((ext_vector_type(4)));
typedef __bf16 v8b __attribute__((ext_vector_type(8)));
typedef __bf16 v2b __attribute__((ext_vector_type(2)));
typedef _Float16 v8h __attribute__((ext_vector_type(8)));
typedef _Float16 v2h __attribute__((ext_vector_type(2)));
typedef float v16f __attribute__((ext_vector_type(16)));
typedef float v4f __attribute__((ext_vector_type(4)));
typedef float v2f __attribute__((ext_vector_type(2)));
typedef unsigned v4u __attribute__((ext_vector_type(4)));
typedef unsigned v2u __attribute__((ext_vector_type(2)));
typedef unsigned long long u64;
typedef __amdgpu_buffer_rsrc_t rsrc_t;

constexpr int SEQ = 2048, DM = 640, NM = 192, FF = 1536;

constexpr unsigned WS_EP = 0;
constexpr unsigned WS_F1 = 4096;
constexpr unsigned WS_HF = 32768;
constexpr unsigned WS_SF = 49152;
constexpr unsigned WS_STATS = 65536;
constexpr unsigned WS_XNT = 131072;
constexpr unsigned WS_W2P = 3u << 20;
constexpr unsigned WS_H = 5u << 20;
constexpr unsigned WS_S = 12u << 20;
constexpr unsigned WS_STAMP = 15u << 20;
constexpr unsigned WS_USED = 16u << 20;

constexpr int XB = 128 * 128, WROW = 192, WB = 64 * WROW, SBY = XB + WB;
constexpr int L_RING = 0;
constexpr int L_EX = L_RING, L_PT = L_RING + 20480, PTS = 176;
constexpr int L_ST = L_RING + 102400;
constexpr int L_TAB = 4 * SBY;
constexpr int USTR = 4696, UPH = 1152;
constexpr int L_U = L_TAB + 3 * 8192;
constexpr int L_PACK = L_U + 3 * USTR + 8;
constexpr int L_CS = L_PACK + 5120;
constexpr int L_CTL = L_CS + 1536;
constexpr int L_END = L_CTL + 256;
static_assert(L_PACK % 16 == 0 && L_CTL % 16 == 0, "alignment");
static_assert(L_END <= 163840, "LDS budget");
constexpr int C_FULL1 = 0, C_FREE1 = 16, C_FULL2 = 32, C_FREE2 = 48, C_LN = 64, C_CS = 80, C_RED = 96, C_P = 112;
constexpr int C_TAG = 128, C_SS = 144, C_U = 148, C_W2 = 152;
constexpr int SPIN = 1 << 18, GSPIN = 1 << 15;
constexpr int SC1 = 16;

struct Args {
  const unsigned short *x, *loga, *aim, *cre, *cim, *ldt, *dsk, *w1, *b1, *w2, *b2;
  unsigned short* out;
  char* ws;
};

__device__ __forceinline__ float u2f(unsigned v) { return __builtin_bit_cast(float, v); }
__device__ __forceinline__ float i2f(int v) { return __builtin_bit_cast(float, v); }
__device__ __forceinline__ unsigned f2u(float v) { return __builtin_bit_cast(unsigned, v); }
__device__ __forceinline__ int f2i(float v) { return __builtin_bit_cast(int, v); }
template <bool S>
__device__ __forceinline__ void glds16(unsigned voff, const void* sbase, unsigned lds_dst) {
  if (S) asm volatile("s_mov_b32 m0, %2\n\ts_nop 0\n\tglobal_load_lds_dwordx4 %0, %1 sc1" :: "v"(voff), "s"(sbase), "s"(lds_dst) : "memory");
  else asm volatile("s_mov_b32 m0, %2\n\ts_nop 0\n\tglobal_load_lds_dwordx4 %0, %1" :: "v"(voff), "s"(sbase), "s"(lds_dst) : "memory");
}
template <bool S>
__device__ __forceinline__ void gld128(v4u& d, unsigned voff, const void* sb) {
  if (S) asm volatile("global_load_dwordx4 %0, %1, %2 sc1" : "+v"(d) : "v"(voff), "s"(sb) : "memory");
  else asm volatile("global_load_dwordx4 %0, %1, %2" : "+v"(d) : "v"(voff), "s"(sb) : "memory");
}
__device__ __forceinline__ void gld64(v2u& d, unsigned voff, const void* sb) {
  asm volatile("global_load_dwordx2 %0, %1, %2" : "+v"(d) : "v"(voff), "s"(sb) : "memory");
}
__device__ __forceinline__ void gld16(unsigned& d, unsigned voff, const void* sb) {
  asm volatile("global_load_ushort %0, %1, %2" : "+v"(d) : "v"(voff), "s"(sb) : "memory");
}
template <int N> __device__ __forceinline__ void vmwait() { asm volatile("s_waitcnt vmcnt(%0)" :: "n"(N) : "memory"); }
#define PIN(v) asm volatile("" : "+v"(v))
#define SB0() __builtin_amdgcn_sched_barrier(0)
template <int... I, class F> __device__ __forceinline__ void sfor_seq(std::integer_sequence<int, I...>, F&& f) { (f(std::integral_constant<int, I>{}), ...); }
template <int N, class F> __device__ __forceinline__ void sfor(F&& f) { sfor_seq(std::make_integer_sequence<int, N>{}, f); }
__device__ __forceinline__ rsrc_t mkrs(const void* p, unsigned n) { return __builtin_amdgcn_make_buffer_rsrc((void*)p, 0, n, 0x00020000); }
template <bool S> __device__ __forceinline__ v4u bld128(rsrc_t r, unsigned off) { return __builtin_amdgcn_raw_buffer_load_b128(r, off, 0, S ? SC1 : 0); }
template <bool S> __device__ __forceinline__ void bst128(rsrc_t r, unsigned off, v4u v) { __builtin_amdgcn_raw_buffer_store_b128(v, r, off, 0, S ? SC1 : 0); }
template <bool S> __device__ __forceinline__ void bst64(rsrc_t r, unsigned off, v2u v, unsigned soff = 0) { __builtin_amdgcn_raw_buffer_store_b64(v, r, off, soff, S ? SC1 : 0); }

__device__ __forceinline__ float bf2f(unsigned v) { return u2f(v << 16); }
__device__ __forceinline__ float bflo(unsigned v) { return u2f(v << 16); }
__device__ __forceinline__ float bfhi(unsigned v) { return u2f(v & 0xffff0000u); }
__device__ __forceinline__ unsigned pk_bf16(float a, float b) { v2f x = {a, b}; v2b r = __builtin_convertvector(x, v2b); return __builtin_bit_cast(unsigned, r); }
__device__ __forceinline__ unsigned pk_f16(float a, float b) { v2f x = {a, b}; v2h r = __builtin_convertvector(x, v2h); return __builtin_bit_cast(unsigned, r); }
__device__ __forceinline__ float rbf(float a) { return u2f(pk_bf16(a, 0.f) << 16); }
__device__ __forceinline__ unsigned rfl(unsigned v) { return __builtin_amdgcn_readfirstlane(v); }
template <int C> __device__ __forceinline__ float dppf(float v) { return i2f(__builtin_amdgcn_update_dpp(0, f2i(v), C, 0xf, 0xf, true)); }
__device__ __forceinline__ float rowsum16(float v) { v += dppf<0x128>(v); v += dppf<0x124>(v); v += dppf<0x122>(v); v += dppf<0x121>(v); return v; }
__device__ __forceinline__ float xor16(float v) { return i2f(__builtin_amdgcn_ds_swizzle(f2i(v), 0x401F)); }
__device__ __forceinline__ float halfsum(float v) {
  const auto r = __builtin_amdgcn_permlane32_swap(f2u(v), f2u(v), false, false);
  return u2f(r[0]) + u2f(r[1]);
}
__device__ __forceinline__ v2u trd(unsigned a) { v4s r = __builtin_amdgcn_ds_read_tr16_b64_v4i16((LAS v4s*)(size_t)a); return __builtin_bit_cast(v2u, r); }
__device__ __forceinline__ v4u tr2(unsigned a, unsigned b) { v2u lo = trd(a), hi = trd(b); return v4u{lo[0], lo[1], hi[0], hi[1]}; }
__device__ __forceinline__ v4u lds128(unsigned a) { return *(const LAS v4u*)(size_t)a; }
__device__ __forceinline__ v2u lds64(unsigned a) { return *(const LAS v2u*)(size_t)a; }
__device__ __forceinline__ float lds32f(unsigned a) { return *(const LAS float*)(size_t)a; }
__device__ __forceinline__ void st128(unsigned a, v4u v) { *(LAS v4u*)(size_t)a = v; }
__device__ __forceinline__ void st64(unsigned a, v2u v) { *(LAS v2u*)(size_t)a = v; }
__device__ __forceinline__ void st32(unsigned a, unsigned v) { *(LAS unsigned*)(size_t)a = v; }
__device__ __forceinline__ void st16(unsigned a, unsigned v) { *(LAS unsigned short*)(size_t)a = (unsigned short)v; }
__device__ __forceinline__ v16f mfma32(v4u a, v4u b, v16f c) { return __builtin_amdgcn_mfma_f32_32x32x16_bf16(__builtin_bit_cast(v8b, a), __builtin_bit_cast(v8b, b), c, 0, 0, 0); }
__device__ __forceinline__ v4f mfma_h(v4u a, v4u b, v4f c) { return __builtin_amdgcn_mfma_f32_16x16x32_f16(__builtin_bit_cast(v8h, a), __builtin_bit_cast(v8h, b), c, 0, 0, 0); }
__device__ __forceinline__ float dot1(unsigned a, float c) { return __builtin_amdgcn_fdot2_f32_bf16(__builtin_bit_cast(v2b, a), __builtin_bit_cast(v2b, 0x3f803f80u), c, false); }

__device__ __forceinline__ void block_sync() { asm volatile("s_waitcnt lgkmcnt(0)\n\ts_barrier" ::: "memory"); }
__device__ __forceinline__ void post(unsigned addr, unsigned val) {
  asm volatile("s_mov_b64 exec, 1\n\tds_write_b32 %0, %1\n\ts_mov_b64 exec, -1" :: "v"(addr), "v"(val) : "memory");
}
__device__ __forceinline__ v4u peek4(unsigned a) { return *(volatile LAS v4u*)(size_t)a; }
__device__ __forceinline__ v2u peek2(unsigned a) { return *(volatile LAS v2u*)(size_t)a; }
__device__ __forceinline__ unsigned peek1(unsigned a) { return *(volatile LAS unsigned*)(size_t)a; }
__device__ __forceinline__ unsigned look1(unsigned a) { return __hip_atomic_load((LAS unsigned*)(size_t)a, __ATOMIC_RELAXED, __HIP_MEMORY_SCOPE_WORKGROUP); }
__device__ __forceinline__ u64 look2(unsigned a) { return __hip_atomic_load((LAS u64*)(size_t)a, __ATOMIC_RELAXED, __HIP_MEMORY_SCOPE_WORKGROUP); }
__device__ __forceinline__ unsigned min2(u64 f) { return min((unsigned)f, (unsigned)(f >> 32)); }
__device__ __forceinline__ unsigned min4(v4u f) { return rfl(min(min(f[0], f[1]), min(f[2], f[3]))); }
#define SLP 0
__device__ __forceinline__ void nap() { if (SLP) __builtin_amdgcn_s_sleep(SLP); }
__device__ __forceinline__ void wait4(unsigned a, unsigned tgt) { for (int i = 0; i < SPIN; ++i) { if (min4(peek4(a)) >= tgt) break; nap(); } }
__device__ __forceinline__ void wait2(unsigned a, unsigned tgt) { for (int i = 0; i < SPIN; ++i) { v2u f = peek2(a); if (rfl(min(f[0], f[1])) >= tgt) break; nap(); } }
__device__ __forceinline__ void wait1(unsigned a, unsigned tgt) { for (int i = 0; i < SPIN; ++i) { if (rfl(peek1(a)) >= tgt) break; nap(); } }

struct Tag { unsigned a, b; };
__device__ __forceinline__ Tag get_tag(unsigned lds) {
  wait1(lds + L_CTL + C_TAG, 1);
  const v4u t = peek4(lds + L_CTL + C_TAG);
  return Tag{rfl(t[1]), rfl(t[2])};
}
__device__ __forceinline__ bool all_lanes(bool ok) { return __builtin_amdgcn_ballot_w64(!ok) == 0; }
template <int NV>
__device__ __forceinline__ void poll_flags(rsrc_t ws, unsigned off, Tag t) {
  for (int i = 0; i < GSPIN; ++i) {
    bool ok = true;
#pragma unroll
    for (int k = 0; k < NV; ++k) { const v4u f = bld128<true>(ws, off + k * 1024); ok = ok && f[0] == t.a && f[1] == t.b && f[2] == t.a && f[3] == t.b; }
    if (all_lanes(ok)) break;
    __builtin_amdgcn_s_sleep(1);
  }
}

struct cf { float r, i; };
__device__ __forceinline__ cf cmul(cf a, cf b) { return cf{a.r * b.r - a.i * b.i, a.r * b.i + a.i * b.r}; }
__device__ __forceinline__ cf cexpp(float a2, float brev, float p) {
  const float m = __builtin_amdgcn_exp2f(a2 * p), f = __builtin_amdgcn_fractf(brev * p);
  return cf{m * __builtin_amdgcn_cosf(f), m * __builtin_amdgcn_sinf(f)};
}

#define STAMP(k) do {} while (0)
#define FSTAMP(k) do {} while (0)

struct LnIn { v2u d[5]; };
__device__ __forceinline__ void ln_load(LnIn& in, const Args& A, int u, int w, int lane) {
  const char* row = (const char*)A.x + (size_t)(8 * u + 2 * w + (lane >> 5)) * (DM * 2) + (lane & 31) * 8;
#pragma unroll
  for (int c = 0; c < 5; ++c) in.d[c] = *(const v2u*)(row + c * 256);
}
template <bool FUSED>
__device__ __forceinline__ void ln_compute(const LnIn& in, const Args& A, int u, int w, int lane, unsigned lds) {
  const int tk = 2 * w + (lane >> 5), i = lane & 31;
  float v[20];
#pragma unroll
  for (int c = 0; c < 5; ++c) { v[4 * c] = bflo(in.d[c][0]); v[4 * c + 1] = bfhi(in.d[c][0]); v[4 * c + 2] = bflo(in.d[c][1]); v[4 * c + 3] = bfhi(in.d[c][1]); }
  float s = 0;
#pragma unroll
  for (int e = 0; e < 20; ++e) s += v[e];
  s = rowsum16(s); s += xor16(s);
  const float mean = s / (float)DM;
  float q = 0;
#pragma unroll
  for (int e = 0; e < 20; ++e) { const float t = v[e] - mean; q += t * t; }
  q = rowsum16(q); q += xor16(q);
  const float m = rbf(mean), sd = rbf(sqrtf(rbf(rbf(q / (float)DM) + 1e-5f))), rs = 1.0f / sd;
  const unsigned dst = lds + L_U + tk * 1280 + i * 8;
#pragma unroll
  for (int c = 0; c < 5; ++c) {
    float o[4];
#pragma unroll
    for (int e = 0; e < 4; ++e) o[e] = rbf(rbf(v[4 * c + e] - m) * rs);
    st64(dst + c * 256, v2u{pk_f16(o[0], o[1]), pk_f16(o[2], o[3])});
  }
  post(lds + L_CTL + C_LN + w * 4, 1);
  Tag t = {0, 0};
  if (FUSED) t = get_tag(lds);
  if (i == 0) {
    const unsigned ms = (f2u(m) >> 16) | (f2u(sd) & 0xffff0000u);
    bst128<FUSED>(mkrs(A.ws, WS_USED), WS_STATS + (8 * u + tk) * 16, v4u{t.a, ms, t.b, ms});
  }
}

struct PackIn { v4u d[5]; };
__device__ __forceinline__ void pack_load(PackIn& in, const Args& A, int unit, int lane) {
  const int s = unit >> 3, nt = unit & 7;
#pragma unroll
  for (int i = 0; i < 5; ++i) {
    const int ci = i * 64 + lane, row = ci / 10, c = ci % 10;
    in.d[i] = *(const v4u*)((const char*)A.w2 + (size_t)(32 * s + row) * (DM * 2) + nt * 160 + c * 16);
  }
}
template <bool FUSED>
__device__ __forceinline__ void pack_store(const PackIn& in, rsrc_t ws, int unit, int lane, unsigned lds) {
  const int s = unit >> 3, nt = unit & 7;
#pragma unroll
  for (int i = 0; i < 5; ++i) st128(lds + L_PACK + i * 1024 + lane * 16, in.d[i]);
  const int q = (lane & 15) >> 2, p = lane & 3, g = lane >> 4;
  const unsigned a0 = lds + L_PACK + (16 * (g >> 1) + 4 * (g & 1) + q) * 160 + p * 8;
  const unsigned dst = WS_W2P + (nt * 48 + s) * 5120 + (g >> 1) * 2560 + (g & 1) * 1280 + (lane & 15) * 16;
#pragma unroll
  for (int ct = 0; ct < 5; ++ct) bst128<FUSED>(ws, dst + ct * 256, tr2(a0 + ct * 32, a0 + 8 * 160 + ct * 32));
}
template <bool FUSED>
__device__ __forceinline__ void ln_publish(rsrc_t ws, int u, int lane, unsigned lds) {
  wait4(lds + L_CTL + C_LN, 1);
  const int q = (lane & 15) >> 2, p = lane & 3, g = lane >> 4;
  const unsigned a0 = lds + L_U + ((q >> 1) + 4 * (q & 1)) * 1280 + g * 32 + p * 8;
#pragma unroll
  for (int i = 0; i < 10; ++i) bst128<FUSED>(ws, WS_XNT + u * 10240 + lane * 16 + i * 1024, tr2(a0 + i * 128, a0 + 2 * 1280 + i * 128));
}
template <bool FUSED>
__device__ __forceinline__ void u_stage(rsrc_t ws, int ch0, int nch, int lane, unsigned lds) {
  const int c = lane & 3, cc = c < nch ? c : 0;
  v4u pc[16];
#pragma unroll
  for (int i = 0; i < 16; ++i) pc[i] = bld128<FUSED>(ws, WS_XNT + (i * 16 + (lane >> 2)) * 10240 + (ch0 + cc) * 16);
  if (lane < 32) {
#pragma unroll
    for (int k = 0; k < 12; ++k) st32(lds + L_U + (k >> 2) * USTR + (k & 3) * UPH + lane * 4, 0);
  }
  if (c < nch) {
#pragma unroll
    for (int i = 0; i < 16; ++i)
#pragma unroll
      for (int r = 0; r < 4; ++r) st32(lds + L_U + c * USTR + r * UPH + 128 + (i * 16 + (lane >> 2)) * 4, pc[i][r]);
  }
  post(lds + L_CTL + C_U, 1);
}
template <bool FUSED>
__device__ __forceinline__ void s_stage(rsrc_t ws, int mt2, int nt2, int lane, unsigned lds) {
  v4u d[10];
#pragma unroll
  for (int i = 0; i < 10; ++i) d[i] = bld128<FUSED>(ws, WS_S + (mt2 * DM + 80 * nt2) * 128 + i * 1024 + lane * 16);
#pragma unroll
  for (int i = 0; i < 10; ++i) st128(lds + L_ST + i * 1024 + lane * 16, d[i]);
  post(lds + L_CTL + C_SS, 1);
}

struct G1Ld { unsigned voff[7], loff[7]; };
__device__ __forceinline__ void g1_issue(const Args& A, const G1Ld& L, int s, unsigned lds) {
  const char* xb = (const char*)A.x + s * 128;
  const char* wb = (const char*)A.w1 + (size_t)s * (64 * FF * 2);
  const unsigned slot = lds + L_RING + (s & 3) * SBY;
#pragma unroll
  for (int i = 0; i < 7; ++i) glds16<false>(L.voff[i], i < 4 ? xb : wb, slot + L.loff[i]);
}
__device__ __forceinline__ void g1_load_begin(G1Ld& L, const Args& A, int mt, int nt, int lw, int lane, unsigned lds) {
#pragma unroll
  for (int i = 0; i < 4; ++i) {
    const int row = 32 * lw + 8 * i + (lane >> 3), c = (lane & 7) ^ ((row >> 1) & 7);
    L.voff[i] = (unsigned)((mt * 128 + row) * (DM * 2) + c * 16);
    L.loff[i] = (4 * lw + i) * 1024;
  }
#pragma unroll
  for (int i = 0; i < 3; ++i) {
    const int ci = (3 * lw + i) * 64 + lane, row = ci / 12, p = ci % 12;
    L.voff[4 + i] = (unsigned)(row * (FF * 2) + nt * WROW + p * 16);
    L.loff[4 + i] = XB + (3 * lw + i) * 1024;
  }
  g1_issue(A, L, 0, lds);
}
__device__ __forceinline__ void g1_load(const G1Ld& L, const Args& A, int lw, unsigned lds) {
  const unsigned full = lds + L_CTL + C_FULL1 + lw * 4, freew = lds + L_CTL + C_FREE1;
#pragma unroll
  for (int s = 1; s < 4; ++s) g1_issue(A, L, s, lds);
  STAMP(3);
  vmwait<21>(); post(full, 1);
  STAMP(4);
#pragma unroll 1
  for (int s = 1; s < 7; ++s) {
    vmwait<14>(); post(full, s + 1);
    FSTAMP(8 + s);
    wait4(freew, s);
    FSTAMP(20 + s);
    g1_issue(A, L, s + 3, lds);
  }
  vmwait<14>(); post(full, 8);
  FSTAMP(15);
  vmwait<7>(); post(full, 9);
  FSTAMP(16);
  vmwait<0>(); post(full, 10);
  FSTAMP(17);
}

template <int W>
__device__ __forceinline__ void g1_main(const Args& A, v16f (&acc)[3], float (&cs)[3], v4u& stat, const char* statp, unsigned statoff, bool fused,
                                        int lane, unsigned lds) {
  const int hi = lane >> 5, G = lane >> 4, q = (lane & 15) >> 2, p = lane & 3;
  unsigned xa[4], wa;
  {
    const int row = 32 * W + (lane & 31), sw = (row >> 1) & 7;
#pragma unroll
    for (int kk = 0; kk < 4; ++kk) xa[kk] = lds + L_RING + row * 128 + (((2 * kk + hi) ^ sw) * 16);
    wa = lds + L_RING + XB + (8 * hi + q) * WROW + (G & 1) * 32 + p * 8;
  }
  const unsigned full = lds + L_CTL + C_FULL1, freew = lds + L_CTL + C_FREE1 + W * 4;
  v4u a[2][3], b[4];
  auto rda = [&](int set, int kk, int t) {
    const unsigned ad = wa + kk * 16 * WROW + t * 64;
    a[set][t] = tr2(ad, ad + 4 * WROW);
  };
  auto step = [&](int kc, int kn) {
    b[kn] = lds128(xa[kn]);
#pragma unroll
    for (int t = 0; t < 3; ++t) {
      acc[t] = mfma32(a[kc & 1][t], b[kc], acc[t]);
      if (kc == W) {
#pragma unroll
        for (int e = 0; e < 4; ++e) cs[t] = dot1(a[kc & 1][t][e], cs[t]);
      }
      rda(kc & 1, kn, t);
      SB0();
    }
  };
  wait4(full, 1);
#pragma unroll
  for (int kk = 0; kk < 2; ++kk) {
    b[kk] = lds128(xa[kk]);
#pragma unroll
    for (int t = 0; t < 3; ++t) rda(kk, kk, t);
  }
  SB0();
#pragma unroll 1
  for (int s = 0; s < 10; ++s) {
    FSTAMP(8 + s);
    const u64 fw0 = look2(full), fw1 = look2(full + 8);
    if (s == 6) { if (fused) gld128<true>(stat, statoff, statp); else gld128<false>(stat, statoff, statp); }
    step(0, 2);
    step(1, 3);
    post(freew, s + 1);
    const unsigned need = s + 2 > 10 ? 10 : s + 2;
    if (rfl(min(min2(fw0), min2(fw1))) < need) wait4(full, need);
    const unsigned delta = (s & 3) == 3 ? (unsigned)(-3 * SBY) : (unsigned)SBY;
#pragma unroll
    for (int kk = 0; kk < 4; ++kk) xa[kk] += delta;
    wa += delta;
    SB0();
    step(2, 0);
    step(3, 1);
  }
}

template <bool FUSED>
__device__ __forceinline__ void g1_compute(const Args& A, int mt, int nt, int w, int lane, unsigned lds) {
  const int tk = lane & 31, hi = lane >> 5;
  unsigned bb[3] = {0, 0, 0};
#pragma unroll
  for (int t = 0; t < 3; ++t) gld16(bb[t], (unsigned)((nt * 96 + 32 * t + tk) * 2), A.b1);
  v4u stat = {0, 0, 0, 0};
  const unsigned statoff = WS_STATS + (mt * 128 + 32 * w + tk) * 16;
  v16f acc[3];
  float cs[3] = {0.f, 0.f, 0.f};
#pragma unroll
  for (int t = 0; t < 3; ++t)
#pragma unroll
    for (int e = 0; e < 16; ++e) acc[t][e] = 0.f;
  if (w == 0) g1_main<0>(A, acc, cs, stat, A.ws, statoff, FUSED, lane, lds);
  else if (w == 1) g1_main<1>(A, acc, cs, stat, A.ws, statoff, FUSED, lane, lds);
  else if (w == 2) g1_main<2>(A, acc, cs, stat, A.ws, statoff, FUSED, lane, lds);
  else g1_main<3>(A, acc, cs, stat, A.ws, statoff, FUSED, lane, lds);
  STAMP(4);
#pragma unroll
  for (int t = 0; t < 3; ++t) {
    const float v = halfsum(cs[t]);
    if (hi == 0) st32(lds + L_CS + (w * 96 + 32 * t + tk) * 4, f2u(v));
  }
  post(lds + L_CTL + C_CS + w * 4, 1);
  vmwait<0>();
  PIN(stat); PIN(bb[0]); PIN(bb[1]); PIN(bb[2]);
  const rsrc_t ws = mkrs(A.ws, WS_USED);
  Tag tag = {0, 0};
  if (FUSED) {
    tag = get_tag(lds);
    for (int i = 0; i < GSPIN; ++i) {
      if (all_lanes(stat[0] == tag.a && stat[2] == tag.b)) break;
      stat = bld128<true>(ws, statoff);
    }
  }
  const unsigned nm = (stat[1] & 0xffffu) ^ 0x8000u, sdb = stat[1] >> 16;
  const v4u baug = {nm | (nm << 16), nm | (sdb << 16), 0, 0};
  const float rstd = 1.0f / bfhi(stat[1]);
  wait4(lds + L_CTL + C_CS, 1);
#pragma unroll
  for (int t = 0; t < 3; ++t) {
    const unsigned ca = lds + L_CS + (32 * t + tk) * 4;
    const float c = (lds32f(ca) + lds32f(ca + 384)) + (lds32f(ca + 768) + lds32f(ca + 1152));
    const float c0 = rbf(c), r1 = c - c0, c1 = rbf(r1), r2 = r1 - c1;
    const unsigned a0 = (f2u(c0) >> 16) | (f2u(c1) & 0xffff0000u), a1 = (pk_bf16(r2, 0.f) & 0xffffu) | (bb[t] << 16);
    acc[t] = mfma32(v4u{hi ? 0u : a0, hi ? 0u : a1, 0, 0}, baug, acc[t]);
  }
  STAMP(5);
#pragma unroll
  for (int t = 0; t < 3; ++t)
#pragma unroll
    for (int hf = 0; hf < 2; ++hf) {
      unsigned pk[4];
#pragma unroll
      for (int e = 0; e < 4; ++e) {
        const float v0 = fmaxf(acc[t][8 * hf + 2 * e], 0.f) * rstd, v1 = fmaxf(acc[t][8 * hf + 2 * e + 1], 0.f) * rstd;
        pk[e] = pk_bf16(v0 * v0, v1 * v1);
      }
      bst128<FUSED>(ws, WS_H + ((mt * 4 + w) * 96 + nt * 6 + 2 * t + hf) * 1024 + lane * 16, v4u{pk[0], pk[1], pk[2], pk[3]});
    }
  if (FUSED) {
    vmwait<0>();
    if (lane == 0) bst64<true>(ws, WS_HF + ((mt * 4 + w) * 2 + (nt >> 3)) * 128 + (nt & 7) * 8, v2u{tag.a, tag.b});
  }
}

struct CvIn { unsigned la[3], ai[3], cr[3], ci[3], ldt, dsk; };
__device__ __forceinline__ void conv_load(CvIn& in, const Args& A, int ch, int lane) {
#pragma unroll
  for (int p = 0; p < 3; ++p) {
    const int n = ch * NM + p * 64 + lane;
    in.la[p] = A.loga[n]; in.ai[p] = A.aim[n]; in.cr[p] = A.cre[n]; in.ci[p] = A.cim[n];
  }
  in.ldt = A.ldt[ch]; in.dsk = A.dsk[ch];
}

template <bool FUSED>
__device__ __forceinline__ void conv_channel(const CvIn& in, const Args& A, int ch, int c, int lane, unsigned lds) {
  const int t16 = lane & 15, g = lane >> 4, q4 = t16 >> 2, p4 = lane & 3;
  const unsigned tab = lds + L_TAB + c * 8192, ub = lds + L_U + c * USTR;
  const float dt = rbf(expf(bf2f(in.ldt))), dsk = bf2f(in.dsk);
  float bound = 0;
#pragma unroll
  for (int p = 0; p < 3; ++p) bound += fabsf(bf2f(in.cr[p])) + fabsf(bf2f(in.ci[p]));
  bound = rowsum16(bound);
  bound = i2f(__builtin_amdgcn_readlane(f2i(bound), 0)) + i2f(__builtin_amdgcn_readlane(f2i(bound), 16)) +
          i2f(__builtin_amdgcn_readlane(f2i(bound), 32)) + i2f(__builtin_amdgcn_readlane(f2i(bound), 48));
  bound = 2.0f * bound * dt + fabsf(dsk);
  int ex = (int)((f2u(bound) >> 23) & 0xff) - 126;
  ex = ex < -100 ? -100 : (ex > 100 ? 100 : ex);
  const float sigma = u2f((unsigned)(127 + 13 - ex) << 23), isigma = u2f((unsigned)(127 - 13 + ex) << 23);

  STAMP(3);
  v4f kt[3];
#pragma unroll
  for (int a = 0; a < 3; ++a) kt[a] = v4f{0, 0, 0, 0};
  const unsigned wre = tab + lane * 32;
  const unsigned rdb = tab + (g >> 1) * 2048 + (4 * (g & 1) + q4) * 32 + p4 * 8;
  auto put = [&](unsigned base, const cf* v) {
    st128(base, v4u{pk_f16(v[0].r, v[1].r), pk_f16(v[2].r, v[3].r), pk_f16(v[4].r, v[5].r), pk_f16(v[6].r, v[7].r)});
    st128(base + 16, v4u{pk_f16(v[8].r, v[9].r), pk_f16(v[10].r, v[11].r), pk_f16(v[12].r, v[13].r), pk_f16(v[14].r, v[15].r)});
    st128(base + 2048, v4u{pk_f16(v[0].i, v[1].i), pk_f16(v[2].i, v[3].i), pk_f16(v[4].i, v[5].i), pk_f16(v[6].i, v[7].i)});
    st128(base + 2064, v4u{pk_f16(v[8].i, v[9].i), pk_f16(v[10].i, v[11].i), pk_f16(v[12].i, v[13].i), pk_f16(v[14].i, v[15].i)});
  };
  auto fetch = [&](v4u (&f)[4], unsigned base) {
#pragma unroll
    for (int m = 0; m < 4; ++m) f[m] = tr2(base + m * 512, base + m * 512 + 256);
  };
  cf v[16], C0, w1, G;
  auto setup = [&](int p) {
    const unsigned lap = p == 0 ? in.la[0] : (p == 1 ? in.la[1] : in.la[2]), aip = p == 0 ? in.ai[0] : (p == 1 ? in.ai[1] : in.ai[2]);
    const unsigned crp = p == 0 ? in.cr[0] : (p == 1 ? in.cr[1] : in.cr[2]), cip = p == 0 ? in.ci[0] : (p == 1 ? in.ci[1] : in.ci[2]);
    const float a2 = -rbf(expf(bf2f(lap))) * dt * 1.44269504088896341f, brev = bf2f(aip) * dt * 0.15915494309189535f;
    const float cs = 2.0f * sigma * dt;
    C0 = cf{bf2f(crp) * cs, bf2f(cip) * cs};
    w1 = cexpp(a2, brev, 1.f); G = cexpp(a2, brev, 128.f);
    const cf w8 = cexpp(a2, -brev, 8.f);
    v[0] = cf{1.f, 0.f}; v[1] = w8;
#pragma unroll
    for (int j = 2; j < 16; ++j) v[j] = cmul(v[j - 1], w8);
  };
  v4u yf[4], af[4];
  setup(0);
  put(wre + 4096, v);
  fetch(yf, rdb + 4096);
  SB0();
#pragma unroll 1
  for (int p = 0; p < 3; ++p) {
    cf e[8];
    e[0] = cf{1.f, 0.f}; e[1] = w1;
#pragma unroll
    for (int k = 2; k < 8; ++k) e[k] = cmul(e[k - 1], w1);
    cf H = C0;
#pragma unroll
    for (int h = 0; h < 2; ++h) {
      v[8 * h] = H;
#pragma unroll
      for (int k = 1; k < 8; ++k) v[8 * h + k] = cmul(H, e[k]);
      H = cmul(H, G);
    }
    put(wre, v);
    fetch(af, rdb);
    SB0();
#pragma unroll
    for (int h = 0; h < 2; ++h) {
      v[4 * h] = H; v[4 * h + 1] = cmul(H, e[2]); v[4 * h + 2] = cmul(H, e[4]); v[4 * h + 3] = cmul(H, e[6]);
      H = cmul(H, G);
    }
#pragma unroll
    for (int h = 0; h < 4; ++h) { v[8 + 2 * h] = H; v[9 + 2 * h] = cmul(H, e[4]); H = cmul(H, G); }
    SB0();
#pragma unroll
    for (int m = 0; m < 4; ++m) kt[0] = mfma_h(af[m], yf[m], kt[0]);
    put(wre, v);
    fetch(af, rdb);
    SB0();
#pragma unroll
    for (int i = 0; i < 9; ++i) { v[i] = H; if (i < 8) H = cmul(H, G); }
#pragma unroll
    for (int i = 9; i < 16; ++i) v[i] = cf{0.f, 0.f};
    SB0();
#pragma unroll
    for (int m = 0; m < 4; ++m) kt[1] = mfma_h(af[m], yf[m], kt[1]);
    put(wre, v);
    fetch(af, rdb);
    SB0();
    if (p < 2) setup(p + 1);
    SB0();
#pragma unroll
    for (int m = 0; m < 4; ++m) kt[2] = mfma_h(af[m], yf[m], kt[2]);
    if (p < 2) { put(wre + 4096, v); fetch(yf, rdb + 4096); }
    SB0();
  }
  STAMP(4);
  const unsigned kb = tab + 32;
  if (lane < 4) st64(tab + lane * 8, v2u{0, 0});
  {
    if (lane == 0) kt[0][0] += dsk * sigma;
    const int P = 4 * (g & 1) + 128 * (g >> 1) + 8 * t16;
    st64(kb + 2 * P, v2u{pk_f16(kt[0][0], kt[0][1]), pk_f16(kt[0][2], kt[0][3])});
  }
  if (g < 2) {
    const int P0 = 256 + 128 * g + 8 * t16;
    const unsigned a01 = pk_f16(kt[1][0], kt[1][1]), a23 = pk_f16(kt[1][2], kt[1][3]);
    const unsigned v0 = a01 & 0xffffu, v1 = a01 >> 16, v2 = a23 & 0xffffu, v3 = a23 >> 16;
    st128(kb + 2 * P0, v4u{v0 | (v0 << 16), v1 | (v2 << 16), v2 | (v2 << 16), v3 | (v3 << 16)});
  } else {
#pragma unroll
    for (int h = 0; h < 2; ++h) {
      const int W = 512 + 256 * (g - 2) + 128 * h + 8 * t16;
      const unsigned ab = pk_f16(kt[1][2 * h], kt[1][2 * h + 1]), a = ab & 0xffffu, b = ab >> 16;
      st128(kb + 2 * W, v4u{a | (a << 16), a | (b << 16), b | (b << 16), b | (b << 16)});
    }
  }
  unsigned k2[4];
  {
    const unsigned a = pk_f16(kt[2][0], kt[2][1]), b = pk_f16(kt[2][2], kt[2][3]);
    k2[0] = (a & 0xffffu) * 0x10001u; k2[1] = (a >> 16) * 0x10001u; k2[2] = (b & 0xffffu) * 0x10001u; k2[3] = (b >> 16) * 0x10001u;
  }
  if (g < 2) {
#pragma unroll
    for (int e = 0; e < 4; ++e) {
      const int P = 1024 + 128 * (4 * g + e) + 8 * t16;
      if (P != 1024) st64(kb + 2 * (P - 4), v2u{k2[e], k2[e]});
      st64(kb + 2 * P, v2u{k2[e], k2[e]});
    }
  } else if (lane == 32) st64(kb + 2 * 2044, v2u{k2[0], k2[0]});
  if (g < 2) {
    const int P0 = 256 + 128 * g + 8 * t16;
    if (P0 != 256) st16(kb + 2 * (P0 - 1), pk_f16(kt[1][0], 0.f));
#pragma unroll
    for (int e = 0; e < 4; ++e) {
      const int P = 1024 + 128 * (4 * g + e) + 8 * t16;
      if (!(t16 & 1)) st16(kb + 2 * (P + 4), k2[e]);
    }
    if (lane == 0) st32(kb + 2 * 1022, k2[0]);
  } else {
#pragma unroll
    for (int h = 0; h < 2; ++h) {
      const int W = 512 + 256 * (g - 2) + 128 * h + 8 * t16;
      const unsigned a = (pk_f16(kt[1][2 * h], 0.f) & 0xffffu) * 0x10001u;
      if (W != 512) st32(kb + 2 * (W - 2), a); else st16(kb + 2 * 511, a);
    }
  }
  v4u T[16];
  {
    const unsigned ta = kb + 2 * (112 - 4 * (8 * g + q4) + 4 * p4);
#pragma unroll
    for (int s = 0; s < 16; ++s) T[s] = tr2(ta + 256 * s, ta + 256 * s - 32);
  }
  STAMP(5);
  wait1(lds + L_CTL + C_U, 1);
  STAMP(6);
  v4f acc[8];
#pragma unroll
  for (int C = 0; C < 8; ++C) acc[C] = v4f{0, 0, 0, 0};
  const int ord = 4 * t16 + g, po = (ord + 63) & 63;
  const int psrc = (16 * (po & 3) + (po >> 2)) * 4;
  const unsigned ua = ub + 2 * (64 - 28 - 32 + 4 * t16 + 8 * g);
  const rsrc_t ws = mkrs(A.ws, WS_USED);
  const unsigned so = WS_S + ((t16 >> 2) * DM + ch) * 128 + (16 * (t16 & 3) + 4 * g) * 2;
  v2u Bl[4], Bh[4];
  float tp[8];
  auto baddr = [&](int n) { return ua + (3 - (n >> 4)) * UPH + 64 * (n & 15); };
  auto carry = [&](int C) { tp[C] = i2f(__builtin_amdgcn_ds_bpermute(psrc, f2i(acc[C][3]))); };
  auto shift = [&](int C) {
    const float first = lane == 0 ? (C == 0 ? 0.f : tp[C > 0 ? C - 1 : 0]) : tp[C];
    float a0 = acc[C][0], a1 = acc[C][1], a2 = acc[C][2];
    asm("" : "+v"(a0), "+v"(a1), "+v"(a2));
    acc[C] = v4f{first, a0, a1, a2};
  };
  auto emit = [&](int C) {
    const v4f y = acc[C];
    bst64<FUSED>(ws, so, v2u{pk_bf16(y[0] * isigma, y[1] * isigma), pk_bf16(y[2] * isigma, y[3] * isigma)}, 4 * C * DM * 128);
  };
#pragma unroll
  for (int n = 0; n < 3; ++n) { Bl[n] = lds64(baddr(n)); SB0(); Bh[n] = lds64(baddr(n) + 8); SB0(); }
  sfor<64>([&](auto N) __attribute__((always_inline)) {
    constexpr int n = N, r = 3 - (n >> 4), q = (n & 15) - 1, c0 = (q + 1) / 2;
    const v4u B = {Bl[n & 3][0], Bl[n & 3][1], Bh[n & 3][0], Bh[n & 3][1]};
    if (q == -1 && r < 3) carry(7);
    sfor<8 - c0>([&](auto M) __attribute__((always_inline)) {
      constexpr int m = M, C = c0 + m;
      if (q == -1 && r < 3 && C >= 6) shift(C);
      acc[C] = mfma_h(T[2 * C - q], B, acc[C]);
      if (n + 3 < 64) {
        if (m == 0) Bl[(n + 3) & 3] = lds64(baddr(n + 3));
        if (m == 1) Bh[(n + 3) & 3] = lds64(baddr(n + 3) + 8);
      }
      if (m == (c0 == 7 ? 0 : 1) && (q & 1) && q > 0) { if (r > 0) carry(q >> 1); else emit(q >> 1); }
      if (m == (c0 >= 6 ? 7 - c0 : 2) && (q & 1) && q >= 3 && r > 0) shift((q >> 1) - 1);
      SB0();
    });
    if (c0 == 7 && n + 3 < 64) { Bh[(n + 3) & 3] = lds64(baddr(n + 3) + 8); SB0(); }
  });
  emit(7);
  STAMP(7);
  if (FUSED) {
    const Tag tag = get_tag(lds);
    vmwait<0>();
    if (lane == 0) bst64<true>(ws, WS_SF + ch * 8, v2u{tag.a, tag.b});
  }
}

template <bool FUSED>
__device__ __forceinline__ void g2_load(const Args& A, int nt2, int rot, int lw, int lane, unsigned lds) {
  const int kh = lw & 1, par = lw >> 1;
  const char* base = A.ws + WS_W2P + (size_t)nt2 * 48 * 5120;
  const unsigned voff = lane * 16;
  auto issue = [&](int n) {
    const int i = 2 * n + par;
    int s = i + rot; s = s >= 24 ? s - 24 : s;
    const char* src = base + (size_t)(24 * kh + s) * 5120;
    const unsigned slot = lds + L_RING + (kh * 10 + (i >= 20 ? i - 20 : (i >= 10 ? i - 10 : i))) * 5120;
#pragma unroll
    for (int c = 0; c < 5; ++c) glds16<false>(voff, src + c * 1024, slot + c * 1024);
  };
  const unsigned full = lds + L_CTL + C_FULL2 + (kh * 2 + par) * 4, freew = lds + L_CTL + C_FREE2 + kh * 8;
  if (FUSED) { wait1(lds + L_CTL + C_W2, 1); wait4(lds + L_CTL + C_FREE1, 10); }
  STAMP(20);
#pragma unroll 1
  for (int n = 0; n < 5; ++n) issue(n);
  STAMP(21);
  vmwait<20>(); post(full, 1);
  STAMP(22);
#pragma unroll 1
  for (int n = 1; n < 8; ++n) {
    vmwait<15>(); post(full, n + 1);
    wait2(freew, 2 * (n - 1) + par + 1);
    issue(n + 4);
  }
  vmwait<15>(); post(full, 9);
  vmwait<10>(); post(full, 10);
  vmwait<5>(); post(full, 11);
  vmwait<0>(); post(full, 12);
}

template <int G>
__device__ __forceinline__ v4f g2_group(const v16f (&acc)[3]) {
  constexpr int t = G < 4 ? 0 : (G < 8 ? 1 : 2), o = G < 4 ? 4 * G : (G < 8 ? 4 * (G - 4) : 8 + 4 * (G - 8));
  return v4f{acc[t][o], acc[t][o + 1], acc[t][o + 2], acc[t][o + 3]};
}
template <int KH, int I>
__device__ __forceinline__ void g2_send(const v16f (&acc)[3], unsigned dst) {
  st128(dst + I * 1024, __builtin_bit_cast(v4u, g2_group<5 * (1 - KH) + I>(acc)));
  if constexpr (I < 4) g2_send<KH, I + 1>(acc, dst);
}
template <int KH, int I>
__device__ __forceinline__ void g2_gate(const v16f (&acc)[3], const v2u (&bv)[5], unsigned exs, unsigned sa, unsigned pa) {
  constexpr int g = 5 * KH + I;
  const v4f o = g2_group<g>(acc), e = __builtin_bit_cast(v4f, lds128(exs + I * 1024));
  const v2u sv = trd(sa + g * 8 * 128);
  const float b4[4] = {bflo(bv[I][0]), bfhi(bv[I][0]), bflo(bv[I][1]), bfhi(bv[I][1])};
  const float s4[4] = {bflo(sv[0]), bfhi(sv[0]), bflo(sv[1]), bfhi(sv[1])};
  float r[4];
#pragma unroll
  for (int k = 0; k < 4; ++k) {
    const float v = o[k] + e[k] + b4[k];
    r[k] = s4[k] * (v * __builtin_amdgcn_rcpf(1.0f + __builtin_amdgcn_exp2f(v * -1.44269504088896341f)));
  }
  st64(pa + g * 16, v2u{pk_bf16(r[0], r[1]), pk_bf16(r[2], r[3])});
  if constexpr (I < 4) g2_gate<KH, I + 1>(acc, bv, exs, sa, pa);
}

template <bool FUSED>
__device__ __forceinline__ void g2_compute(const Args& A, int mt2, int nt2, int rot, int w, int lane, unsigned lds) {
  const int th = w & 1, kh = w >> 1, hi = lane >> 5, T32 = 2 * mt2 + th;
  if (FUSED) poll_flags<1>(mkrs(A.ws, WS_USED), WS_HF + (T32 * 2 + kh) * 128 + (lane & 3) * 16, get_tag(lds));
  STAMP(20);
  const char* hbase = A.ws + WS_H + (size_t)(T32 * 96 + 48 * kh) * 1024;
  const unsigned voff = lane * 16;
  v4u hb[6][2], wa[3], wb[3];
  v16f acc[3];
#pragma unroll
  for (int t = 0; t < 3; ++t)
#pragma unroll
    for (int e = 0; e < 16; ++e) acc[t][e] = 0.f;
  int sl = rot;
  auto hload = [&](int k) {
    const char* p = hbase + sl * 2048;
    gld128<FUSED>(hb[k][0], voff, p); gld128<FUSED>(hb[k][1], voff, p + 1024);
    sl = sl == 23 ? 0 : sl + 1;
  };
#pragma unroll
  for (int k = 0; k < 6; ++k) { hb[k][0] = v4u{0, 0, 0, 0}; hb[k][1] = v4u{0, 0, 0, 0}; hload(k); }
  const unsigned full = lds + L_CTL + C_FULL2 + kh * 8, freew = lds + L_CTL + C_FREE2 + (kh * 2 + th) * 4;
  const unsigned wbase = lds + L_RING + kh * 51200 + hi * 1280 + (lane & 31) * 16;
  constexpr int toff[3] = {0, 512, 768};
  unsigned fw = 0;
  wait1(full, 1);
  STAMP(21);
#pragma unroll
  for (int t = 0; t < 3; ++t) wa[t] = lds128(wbase + toff[t]);
#pragma unroll
  for (int t = 0; t < 3; ++t) wb[t] = lds128(wbase + 2560 + toff[t]);
  post(freew, 1);
  unsigned slot = 0;
  auto body = [&](int i, int k, bool tail) {
    const bool more = !(tail && k == 5);
    const unsigned fwa = full + ((k + 1) & 1) * 4, need = (i + 1) / 2 + 1;
    if (more && rfl(fw) < need) wait1(fwa, need);
    if (more) fw = look1(full + (k & 1) * 4);
    slot = slot == 9 ? 0 : slot + 1;
    const unsigned nxt = wbase + slot * 5120;
    if (!tail) vmwait<10>();
    else if (k == 0) vmwait<10>(); else if (k == 1) vmwait<8>(); else if (k == 2) vmwait<6>();
    else if (k == 3) vmwait<12>(); else if (k == 4) vmwait<10>(); else vmwait<8>();
    PIN(hb[k][0]); PIN(hb[k][1]);
    SB0();
#pragma unroll
    for (int t = 0; t < 3; ++t) { acc[t] = mfma32(wa[t], hb[k][0], acc[t]); if (more) wa[t] = lds128(nxt + toff[t]); SB0(); }
#pragma unroll
    for (int t = 0; t < 3; ++t) { acc[t] = mfma32(wb[t], hb[k][1], acc[t]); if (more) wb[t] = lds128(nxt + 2560 + toff[t]); SB0(); }
    if (more) post(freew, i + 2);
    if (!tail) hload(k);
    SB0();
  };
#pragma unroll 1
  for (int it = 0; it < 3; ++it) {
#pragma unroll
    for (int k = 0; k < 6; ++k) body(it * 6 + k, k, false);
  }
#pragma unroll
  for (int k = 0; k < 3; ++k) body(18 + k, k, true);
  v4u xv[3];
  v2u bv[5];
  auto xoff = [&](int i) {
    const int idx = min(i * 64 + lane, 159);
    return (unsigned)((T32 * 32 + 16 * kh + idx / 10) * (DM * 2) + nt2 * 160 + (idx % 10) * 16);
  };
#pragma unroll
  for (int i = 0; i < 5; ++i) { bv[i] = v2u{0, 0}; gld64(bv[i], (unsigned)((80 * nt2 + 8 * (5 * kh + i) + 4 * hi) * 2), A.b2); }
#pragma unroll
  for (int i = 0; i < 3; ++i) { xv[i] = v4u{0, 0, 0, 0}; gld128<false>(xv[i], xoff(i), A.x); }
  SB0();
#pragma unroll
  for (int k = 3; k < 6; ++k) body(18 + k, k, true);
  STAMP(22);
  wait4(lds + L_CTL + C_FREE2, 24);
  const unsigned exd = lds + L_EX + w * 5120 + lane * 16, exs = lds + L_EX + (w ^ 2) * 5120 + lane * 16;
  if (kh == 0) g2_send<0, 0>(acc, exd); else g2_send<1, 0>(acc, exd);
  post(lds + L_CTL + C_RED + w * 4, 1);
  wait1(lds + L_CTL + C_RED + (w ^ 2) * 4, 1);
  wait1(lds + L_CTL + C_SS, 1);
  STAMP(23);
  const int G = lane >> 4, q = (lane & 15) >> 2, p = lane & 3;
  const unsigned sa = lds + L_ST + (4 * hi + q) * 128 + (32 * th + 16 * (G & 1) + 4 * p) * 2;
  const unsigned pa = lds + L_PT + (32 * th + (lane & 31)) * PTS + 8 * hi;
  vmwait<3>();
#pragma unroll
  for (int i = 0; i < 5; ++i) PIN(bv[i]);
  if (kh == 0) g2_gate<0, 0>(acc, bv, exs, sa, pa); else g2_gate<1, 0>(acc, bv, exs, sa, pa);
  post(lds + L_CTL + C_P + w * 4, 1);
  wait1(lds + L_CTL + C_P + (w ^ 2) * 4, 1);
  STAMP(24);
  vmwait<0>();
#pragma unroll
  for (int i = 0; i < 3; ++i) {
    const int idx = min(i * 64 + lane, 159);
    const v4u pv = lds128(lds + L_PT + (32 * th + 16 * kh + idx / 10) * PTS + (idx % 10) * 16);
    PIN(xv[i]);
    v4u o;
#pragma unroll
    for (int e = 0; e < 4; ++e) o[e] = pk_bf16(bflo(xv[i][e]) + bflo(pv[e]), bfhi(xv[i][e]) + bfhi(pv[e]));
    if (i < 2 || lane < 32) *(v4u*)((char*)A.out + xoff(i)) = o;
  }
}

template <bool FUSED>
__device__ __forceinline__ void aux_wave(const Args& A, int ph, int b, int u, int mt2, int nt2, int lane, unsigned lds) {
  const rsrc_t ws = mkrs(A.ws, WS_USED);
  PackIn p0, p1;
  v2u ep = {0, 0};
  if (FUSED) ep = __builtin_amdgcn_raw_buffer_load_b64(ws, WS_EP, 0, SC1);
  if (ph & 1) {
    pack_load(p0, A, b, lane);
    pack_load(p1, A, b < 128 ? 256 + b : b, lane);
  }
  block_sync();
  Tag tag = {0, 0};
  if (FUSED) {
    tag.a = rfl(ep[0]) ^ 0x5bd1e995u; tag.b = rfl(ep[1]) ^ 0x9e3779b9u;
    st64(lds + L_CTL + C_TAG + 8, v2u{tag.b, 0});
    st32(lds + L_CTL + C_TAG + 4, tag.a);
    post(lds + L_CTL + C_TAG, 1);
  }
  if (ph & 1) {
    pack_store<FUSED>(p0, ws, b, lane, lds);
    if (b < 128) pack_store<FUSED>(p1, ws, 256 + b, lane, lds);
    ln_publish<FUSED>(ws, u, lane, lds);
  }
  STAMP(3);
  if (FUSED) {
    vmwait<0>();
    if (lane < 8) bst64<true>(ws, WS_F1 + lane * 2048 + b * 8, v2u{tag.a, tag.b});
    STAMP(4);
    poll_flags<2>(ws, WS_F1 + (b & 7) * 2048 + lane * 16, tag);
    if (b == 0 && lane == 0) {
      const u64 e = (((u64)ep[1] << 32) | ep[0]) + 1;
      bst64<true>(ws, WS_EP, v2u{(unsigned)e, (unsigned)(e >> 32)});
    }
  }
  STAMP(5);
  if (ph & 2) u_stage<FUSED>(ws, 5 * (b >> 1) + 3 * (b & 1), 3 - (b & 1), lane, lds);
  STAMP(6);
  if (FUSED) {
    __builtin_amdgcn_fence(__ATOMIC_ACQUIRE, "agent");
    vmwait<0>();
    post(lds + L_CTL + C_W2, 1);
    STAMP(7);
    wait4(lds + L_CTL + C_FREE1, 10);
    poll_flags<1>(ws, WS_SF + nt2 * 640 + min(lane, 39) * 16, tag);
  }
  STAMP(8);
  if (ph & 4) s_stage<FUSED>(ws, mt2, nt2, lane, lds);
}

template <int PH>
__global__ __launch_bounds__(768, 3) void s4d_mlp(Args A) {
  constexpr bool FUSED = PH == 7;
  extern __shared__ __attribute__((aligned(16))) char smem[];
  const unsigned lds = (unsigned)(size_t)(LAS char*)smem;
  const int lane = threadIdx.x & 63, wv = __builtin_amdgcn_readfirstlane(threadIdx.x >> 6);
  const int b = blockIdx.x, xcd = b & 7, j = b >> 3;
  STAMP(PH == 1 || PH == 7 ? 0 : PH == 2 ? 2 : 19);
  const int mt = 4 * (xcd >> 1) + (j >> 3), nt = 8 * (xcd & 1) + (j & 7);
  const int mt2 = 4 * xcd + (j >> 3), nt2 = j & 7, rot = (3 * (j >> 3) + 12 * (xcd & 1)) % 24;
  if (wv < 4) {
    LnIn in;
    if (PH & 1) ln_load(in, A, 16 * mt + nt, wv, lane);
    if (wv == 0) st32(lds + L_CTL + lane * 4, 0);
    block_sync();
    if (PH & 1) ln_compute<FUSED>(in, A, 16 * mt + nt, wv, lane, lds);
    STAMP(1);
    if (PH & 2) g1_compute<FUSED>(A, mt, nt, wv, lane, lds);
    STAMP(6);
    if (PH & 4) g2_compute<FUSED>(A, mt2, nt2, rot, wv, lane, lds);
  } else if (wv < 8) {
    G1Ld L;
    if (PH & 2) g1_load_begin(L, A, mt, nt, wv - 4, lane, lds);
    block_sync();
    if (PH & 2) g1_load(L, A, wv - 4, lds);
    STAMP(6);
    if (PH & 4) g2_load<FUSED>(A, nt2, rot, wv - 4, lane, lds);
  } else if (wv < 11) {
    const int c = wv - 8, ch = 5 * (b >> 1) + 3 * (b & 1) + c;
    const bool on = (PH & 2) && c < 3 - (b & 1);
    CvIn in;
    if (on) conv_load(in, A, ch, lane);
    block_sync();
    if (on) conv_channel<FUSED>(in, A, ch, c, lane, lds);
  } else {
    aux_wave<FUSED>(A, PH, b, 16 * mt + nt, mt2, nt2, lane, lds);
  }
  STAMP(31);
}

#if defined(__HIP_DEVICE_COMPILE__)
#pragma clang attribute pop
#endif

extern "C" void kernel_launch(void* const* d_in, const int* in_sizes, int n_in, void* d_out, int out_size, void* d_ws, size_t ws_size, hipStream_t stream) {
  Args A;
  A.x = (const unsigned short*)d_in[0]; A.loga = (const unsigned short*)d_in[1]; A.aim = (const unsigned short*)d_in[2];
  A.cre = (const unsigned short*)d_in[3]; A.cim = (const unsigned short*)d_in[4]; A.ldt = (const unsigned short*)d_in[5];
  A.dsk = (const unsigned short*)d_in[6]; A.w1 = (const unsigned short*)d_in[8]; A.b1 = (const unsigned short*)d_in[9];
  A.w2 = (const unsigned short*)d_in[10]; A.b2 = (const unsigned short*)d_in[11];
  A.out = (unsigned short*)d_out; A.ws = (char*)d_ws;
  (void)hipFuncSetAttribute((const void*)s4d_mlp<7>, hipFuncAttributeMaxDynamicSharedMemorySize, L_END);
  s4d_mlp<7><<<256, 768, L_END, stream>>>(A);
}
```

```cpp
#include <hip/hip_runtime.h>
#include <hip/hip_bf16.h>
#include <stdint.h>
#include <utility>

#if defined(__HIP_DEVICE_COMPILE__)
#pragma clang attribute push (__attribute__((target("no-packed-fp32-ops"))), apply_to=function)
#endif
#define LAS __attribute__((address_space(3)))
typedef short v4s __attribute__((ext_vector_type(4)));
typedef __bf16 v8b __attribute__((ext_vector_type(8)));
typedef __bf16 v2b __attribute__((ext_vector_type(2)));
typedef _Float16 v8h __attribute__((ext_vector_type(8)));
typedef _Float16 v2h __attribute__((ext_vector_type(2)));
typedef float v16f __attribute__((ext_vector_type(16)));
typedef float v4f __attribute__((ext_vector_type(4)));
typedef float v2f __attribute__((ext_vector_type(2)));
typedef unsigned v4u __attribute__((ext_vector_type(4)));
typedef unsigned v2u __attribute__((ext_vector_type(2)));
typedef unsigned long long u64;
typedef __amdgpu_buffer_rsrc_t rsrc_t;

constexpr int SEQ = 2048, DM = 640, NM = 192, FF = 1536;

constexpr unsigned WS_EP = 0;
constexpr unsigned WS_F1 = 4096;
constexpr unsigned WS_HF = 32768;
constexpr unsigned WS_SF = 49152;
constexpr unsigned WS_STATS = 65536;
constexpr unsigned WS_XNT = 131072;
constexpr unsigned WS_H = 5u << 20;
constexpr unsigned WS_S = 12u << 20;
constexpr unsigned WS_USED = 16u << 20;

constexpr int XB = 128 * 128, WROW = 192, WB = 64 * WROW, SBY = XB + WB;
constexpr int L_RING = 0;
constexpr int L_EX = L_RING, L_PT = L_RING + 20480, PTS = 176;
constexpr int L_W2A = L_RING + 63488, L_W2B = L_RING;
constexpr int L_ST = L_RING + 51200;
constexpr int L_TAB = 4 * SBY;
constexpr int USTR = 4696, UPH = 1152;
constexpr int L_U = L_TAB + 3 * 8192;
constexpr int L_CS = L_U + 3 * USTR + 8;
constexpr int L_CTL = L_CS + 1536;
constexpr int L_DUMMY = L_CTL + 256;
constexpr int L_END = L_DUMMY + 256;
constexpr int C_FREE1 = 16, C_FULL2 = 32, C_FREE2 = 48, C_LN = 64, C_CS = 80, C_RED = 96;
constexpr int C_TAG = 128, C_SS = 144, C_U = 148, C_FS = 160, C_HS = 208, C_LI = 224;
constexpr int SPIN = 1 << 18, GSPIN = 1 << 15;
constexpr int SC1 = 16;

struct Args {
  const unsigned short *x, *loga, *aim, *cre, *cim, *ldt, *dsk, *w1, *b1, *w2, *b2;
  unsigned short* out;
  char* ws;
};

__device__ __forceinline__ float u2f(unsigned v) { return __builtin_bit_cast(float, v); }
__device__ __forceinline__ float i2f(int v) { return __builtin_bit_cast(float, v); }
__device__ __forceinline__ unsigned f2u(float v) { return __builtin_bit_cast(unsigned, v); }
__device__ __forceinline__ int f2i(float v) { return __builtin_bit_cast(int, v); }
__device__ __forceinline__ void set_m0(unsigned v) { asm volatile("s_mov_b32 m0, %0\n\ts_nop 0" :: "s"(v) : "memory"); }
template <int OFF>
__device__ __forceinline__ void glds16o(unsigned voff, const void* sbase) {
  asm volatile("global_load_lds_dwordx4 %0, %1 offset:%2" :: "v"(voff), "s"(sbase), "n"(OFF) : "memory");
}
template <bool S>
__device__ __forceinline__ void gld128(v4u& d, unsigned voff, const void* sb) {
  if (S) asm volatile("global_load_dwordx4 %0, %1, %2 sc1" : "+v"(d) : "v"(voff), "s"(sb) : "memory");
  else asm volatile("global_load_dwordx4 %0, %1, %2" : "+v"(d) : "v"(voff), "s"(sb) : "memory");
}
template <bool S, int OFF>
__device__ __forceinline__ void gld128o(v4u& d, unsigned voff, const void* sb) {
  if (S) asm volatile("global_load_dwordx4 %0, %1, %2 offset:%3 sc1" : "+v"(d) : "v"(voff), "s"(sb), "n"(OFF) : "memory");
  else asm volatile("global_load_dwordx4 %0, %1, %2 offset:%3" : "+v"(d) : "v"(voff), "s"(sb), "n"(OFF) : "memory");
}
__device__ __forceinline__ void gld64(v2u& d, unsigned voff, const void* sb) {
  asm volatile("global_load_dwordx2 %0, %1, %2" : "+v"(d) : "v"(voff), "s"(sb) : "memory");
}
template <bool S>
__device__ __forceinline__ void gld64s(v2u& d, unsigned voff, const void* sb) {
  asm volatile("global_load_dwordx2 %0, %1, %2 sc1" : "+v"(d) : "v"(voff), "s"(sb) : "memory");
}
__device__ __forceinline__ void gld32(unsigned& d, unsigned voff, const void* sb) {
  asm volatile("global_load_dword %0, %1, %2" : "+v"(d) : "v"(voff), "s"(sb) : "memory");
}
__device__ __forceinline__ void gld16(unsigned& d, unsigned voff, const void* sb) {
  asm volatile("global_load_ushort %0, %1, %2" : "+v"(d) : "v"(voff), "s"(sb) : "memory");
}
template <int N> __device__ __forceinline__ void vmwait() { asm volatile("s_waitcnt vmcnt(%0)" :: "n"(N) : "memory"); }
#define PIN(v) asm volatile("" : "+v"(v))
#define SB0() __builtin_amdgcn_sched_barrier(0)
#define RARE(c) __builtin_expect(!!(c), 0)
template <int... I, class F> __device__ __forceinline__ void sfor_seq(std::integer_sequence<int, I...>, F&& f) { (f(std::integral_constant<int, I>{}), ...); }
template <int N, class F> __device__ __forceinline__ void sfor(F&& f) { sfor_seq(std::make_integer_sequence<int, N>{}, f); }
__device__ __forceinline__ rsrc_t mkrs(const void* p, unsigned n) { return __builtin_amdgcn_make_buffer_rsrc((void*)p, 0, n, 0x00020000); }
template <bool S> __device__ __forceinline__ v4u bld128(rsrc_t r, unsigned off) { return __builtin_amdgcn_raw_buffer_load_b128(r, off, 0, S ? SC1 : 0); }
template <bool S> __device__ __forceinline__ void bst128(rsrc_t r, unsigned off, v4u v) { __builtin_amdgcn_raw_buffer_store_b128(v, r, off, 0, S ? SC1 : 0); }
template <bool S> __device__ __forceinline__ void bst64(rsrc_t r, unsigned off, v2u v, unsigned soff = 0) { __builtin_amdgcn_raw_buffer_store_b64(v, r, off, soff, S ? SC1 : 0); }

__device__ __forceinline__ float bf2f(unsigned v) { return u2f(v << 16); }
__device__ __forceinline__ float bflo(unsigned v) { return u2f(v << 16); }
__device__ __forceinline__ float bfhi(unsigned v) { return u2f(v & 0xffff0000u); }
__device__ __forceinline__ unsigned pk_bf16(float a, float b) { v2f x = {a, b}; v2b r = __builtin_convertvector(x, v2b); return __builtin_bit_cast(unsigned, r); }
__device__ __forceinline__ unsigned pk_f16(float a, float b) { v2f x = {a, b}; v2h r = __builtin_convertvector(x, v2h); return __builtin_bit_cast(unsigned, r); }
__device__ __forceinline__ float rbf(float a) { return u2f(pk_bf16(a, 0.f) << 16); }
__device__ __forceinline__ unsigned rfl(unsigned v) { return __builtin_amdgcn_readfirstlane(v); }
template <int C> __device__ __forceinline__ float dppf(float v) { return i2f(__builtin_amdgcn_update_dpp(0, f2i(v), C, 0xf, 0xf, true)); }
__device__ __forceinline__ float rowsum16(float v) { v += dppf<0x128>(v); v += dppf<0x124>(v); v += dppf<0x122>(v); v += dppf<0x121>(v); return v; }
__device__ __forceinline__ float xor16(float v) { return i2f(__builtin_amdgcn_ds_swizzle(f2i(v), 0x401F)); }
__device__ __forceinline__ v2u trd(unsigned a) { v4s r = __builtin_amdgcn_ds_read_tr16_b64_v4i16((LAS v4s*)(size_t)a); return __builtin_bit_cast(v2u, r); }
__device__ __forceinline__ v4u tr2(unsigned a, unsigned b) { v2u lo = trd(a), hi = trd(b); return v4u{lo[0], lo[1], hi[0], hi[1]}; }
__device__ __forceinline__ v4u lds128(unsigned a) { return *(const LAS v4u*)(size_t)a; }
__device__ __forceinline__ v2u lds64(unsigned a) { return *(const LAS v2u*)(size_t)a; }
__device__ __forceinline__ float lds32f(unsigned a) { return *(const LAS float*)(size_t)a; }
__device__ __forceinline__ void st128(unsigned a, v4u v) { *(LAS v4u*)(size_t)a = v; }
__device__ __forceinline__ void st64(unsigned a, v2u v) { *(LAS v2u*)(size_t)a = v; }
__device__ __forceinline__ void st32(unsigned a, unsigned v) { *(LAS unsigned*)(size_t)a = v; }
__device__ __forceinline__ void st16(unsigned a, unsigned v) { *(LAS unsigned short*)(size_t)a = (unsigned short)v; }
__device__ __forceinline__ v16f mfma32(v4u a, v4u b, v16f c) { return __builtin_amdgcn_mfma_f32_32x32x16_bf16(__builtin_bit_cast(v8b, a), __builtin_bit_cast(v8b, b), c, 0, 0, 0); }
__device__ __forceinline__ v4f mfma16(v4u a, v4u b, v4f c) { return __builtin_amdgcn_mfma_f32_16x16x32_bf16(__builtin_bit_cast(v8b, a), __builtin_bit_cast(v8b, b), c, 0, 0, 0); }
__device__ __forceinline__ v4f mfma_h(v4u a, v4u b, v4f c) { return __builtin_amdgcn_mfma_f32_16x16x32_f16(__builtin_bit_cast(v8h, a), __builtin_bit_cast(v8h, b), c, 0, 0, 0); }

__device__ __forceinline__ void bar() { asm volatile("s_barrier" ::: "memory"); }
__device__ __forceinline__ void block_sync() { asm volatile("s_waitcnt lgkmcnt(0)\n\ts_barrier" ::: "memory"); }
__device__ __forceinline__ void post(unsigned addr, unsigned val) {
  asm volatile("s_mov_b64 exec, 1\n\tds_write_b32 %0, %1\n\ts_mov_b64 exec, -1" :: "v"(addr), "v"(val) : "memory");
}
__device__ __forceinline__ void count(unsigned addr) {
  asm volatile("s_mov_b64 exec, 1\n\tds_add_u32 %0, %1\n\ts_mov_b64 exec, -1" :: "v"(addr), "v"(1u) : "memory");
}
__device__ __forceinline__ void bump(unsigned addr, unsigned one) {
  (void)__hip_atomic_fetch_add((LAS unsigned*)(size_t)addr, one, __ATOMIC_RELAXED, __HIP_MEMORY_SCOPE_WORKGROUP);
}
__device__ __forceinline__ v4u peek4(unsigned a) { return *(volatile LAS v4u*)(size_t)a; }
__device__ __forceinline__ v2u peek2(unsigned a) { return *(volatile LAS v2u*)(size_t)a; }
__device__ __forceinline__ unsigned peek1(unsigned a) { return *(volatile LAS unsigned*)(size_t)a; }
__device__ __forceinline__ unsigned look1(unsigned a) { return __hip_atomic_load((LAS unsigned*)(size_t)a, __ATOMIC_RELAXED, __HIP_MEMORY_SCOPE_WORKGROUP); }
__device__ __forceinline__ unsigned min4(v4u f) { return rfl(min(min(f[0], f[1]), min(f[2], f[3]))); }
__device__ __forceinline__ void nap() { }
__device__ __forceinline__ void wait4(unsigned a, unsigned tgt) { for (int i = 0; i < SPIN; ++i) { if (min4(peek4(a)) >= tgt) break; nap(); } }
__device__ __forceinline__ void wait2(unsigned a, unsigned tgt) { for (int i = 0; i < SPIN; ++i) { v2u f = peek2(a); if (rfl(min(f[0], f[1])) >= tgt) break; nap(); } }
__device__ __forceinline__ void wait1(unsigned a, unsigned tgt) { for (int i = 0; i < SPIN; ++i) { if (rfl(peek1(a)) >= tgt) break; nap(); } }

struct Tag { unsigned a, b; };
__device__ __forceinline__ Tag get_tag(unsigned lds) {
  wait1(lds + L_CTL + C_TAG, 1);
  const v4u t = peek4(lds + L_CTL + C_TAG);
  return Tag{rfl(t[1]), rfl(t[2])};
}
__device__ __forceinline__ bool all_lanes(bool ok) { return __builtin_amdgcn_ballot_w64(!ok) == 0; }
template <int NV, int NAP = 4>
__device__ __forceinline__ void poll_flags(rsrc_t ws, unsigned off, Tag t) {
#pragma unroll 1
  for (int i = 0; i < GSPIN; ++i) {
    bool ok = true;
#pragma unroll
    for (int k = 0; k < NV; ++k) { const v4u f = bld128<true>(ws, off + k * 1024); ok = ok && f[0] == t.a && f[1] == t.b && f[2] == t.a && f[3] == t.b; }
    if (all_lanes(ok)) break;
    __builtin_amdgcn_s_sleep(NAP);
  }
}

struct cf { float r, i; };
__device__ __forceinline__ cf cmul(cf a, cf b) { return cf{a.r * b.r - a.i * b.i, a.r * b.i + a.i * b.r}; }
__device__ __forceinline__ cf cexpp(float a2, float brev, float p) {
  const float m = __builtin_amdgcn_exp2f(a2 * p), f = __builtin_amdgcn_fractf(brev * p);
  return cf{m * __builtin_amdgcn_cosf(f), m * __builtin_amdgcn_sinf(f)};
}

struct LnIn { v2u d[5]; };
__device__ __forceinline__ void ln_load(LnIn& in, const Args& A, int u, int w, int lane) {
  const char* row = (const char*)A.x + (size_t)(8 * u + 2 * w + (lane >> 5)) * (DM * 2) + (lane & 31) * 8;
#pragma unroll
  for (int c = 0; c < 5; ++c) in.d[c] = *(const v2u*)(row + c * 256);
}
template <bool FUSED>
__device__ __forceinline__ Tag ln_compute(const LnIn& in, const Args& A, int u, int w, int lane, unsigned lds) {
  const int tk = 2 * w + (lane >> 5), i = lane & 31;
  float v[20];
#pragma unroll
  for (int c = 0; c < 5; ++c) { v[4 * c] = bflo(in.d[c][0]); v[4 * c + 1] = bfhi(in.d[c][0]); v[4 * c + 2] = bflo(in.d[c][1]); v[4 * c + 3] = bfhi(in.d[c][1]); }
  float s = 0;
#pragma unroll
  for (int e = 0; e < 20; ++e) s += v[e];
  s = rowsum16(s); s += xor16(s);
  const float mean = s / (float)DM;
  float q = 0;
#pragma unroll
  for (int e = 0; e < 20; ++e) { const float t = v[e] - mean; q += t * t; }
  q = rowsum16(q); q += xor16(q);
  const float m = rbf(mean), sd = rbf(sqrtf(rbf(rbf(q / (float)DM) + 1e-5f))), rs = 1.0f / sd;
  const unsigned dst = lds + L_U + tk * 1280 + i * 8;
#pragma unroll
  for (int c = 0; c < 5; ++c) {
    float o[4];
#pragma unroll
    for (int e = 0; e < 4; ++e) o[e] = rbf(rbf(v[4 * c + e] - m) * rs);
    st64(dst + c * 256, v2u{pk_f16(o[0], o[1]), pk_f16(o[2], o[3])});
  }
  post(lds + L_CTL + C_LN + w * 4, 1);
  Tag t = {0, 0};
  if (FUSED) t = get_tag(lds);
  if (i == 0) {
    const unsigned ms = (f2u(m) >> 16) | (f2u(sd) & 0xffff0000u);
    bst128<FUSED>(mkrs(A.ws, WS_USED), WS_STATS + (8 * u + tk) * 16, v4u{t.a, ms, t.b, ms});
  }
  return t;
}

template <bool FUSED>
__device__ __forceinline__ void ln_publish(rsrc_t ws, int u, int lane, unsigned lds) {
  wait4(lds + L_CTL + C_LN, 1);
  const int q = (lane & 15) >> 2, p = lane & 3, g = lane >> 4;
  const unsigned a0 = lds + L_U + ((q >> 1) + 4 * (q & 1)) * 1280 + g * 32 + p * 8;
#pragma unroll
  for (int i = 0; i < 10; ++i) bst128<FUSED>(ws, WS_XNT + u * 10240 + lane * 16 + i * 1024, tr2(a0 + i * 128, a0 + 2 * 1280 + i * 128));
}
struct UIn { v4u pc[16]; };
template <bool FUSED>
__device__ __forceinline__ void u_load(UIn& in, rsrc_t ws, int ch0, int nch, int lane) {
  const int c = lane & 3, cc = c < nch ? c : 0;
#pragma unroll
  for (int i = 0; i < 16; ++i) in.pc[i] = bld128<FUSED>(ws, WS_XNT + (i * 16 + (lane >> 2)) * 10240 + (ch0 + cc) * 16);
}
__device__ __forceinline__ void u_store(const UIn& in, int nch, int lane, unsigned lds) {
  const int c = lane & 3;
  if (lane < 32) {
#pragma unroll
    for (int k = 0; k < 12; ++k) st32(lds + L_U + (k >> 2) * USTR + (k & 3) * UPH + lane * 4, 0);
  }
  if (c < nch) {
#pragma unroll
    for (int i = 0; i < 16; ++i)
#pragma unroll
      for (int r = 0; r < 4; ++r) st32(lds + L_U + c * USTR + r * UPH + 128 + (i * 16 + (lane >> 2)) * 4, in.pc[i][r]);
  }
  post(lds + L_CTL + C_U, 1);
}
template <bool FUSED>
__device__ __forceinline__ void s_stage(rsrc_t ws, int mt2, int nt2, int lane, unsigned lds) {
  v4u d[10];
#pragma unroll
  for (int i = 0; i < 10; ++i) d[i] = bld128<FUSED>(ws, WS_S + (mt2 * DM + 80 * nt2) * 128 + i * 1024 + lane * 16);
#pragma unroll
  for (int i = 0; i < 10; ++i) st128(lds + L_ST + i * 1024 + lane * 16, d[i]);
  post(lds + L_CTL + C_SS, 1);
}

struct G1Ld { unsigned vx[4], vw[3]; int rot; };
__device__ __forceinline__ void g1_addr(G1Ld& L, int mt, int nt, int lane) {
  L.rot = 0;
#pragma unroll
  for (int i = 0; i < 4; ++i) {
    const int row = 8 * i + (lane >> 3), c = (lane & 7) ^ ((row >> 1) & 7);
    L.vx[i] = (unsigned)((mt * 128 + row) * (DM * 2) + c * 16 - i * 1024);
  }
#pragma unroll
  for (int i = 0; i < 3; ++i) {
    const int ci = i * 64 + lane, row = ci / 12, p = ci % 12;
    L.vw[i] = (unsigned)(row * (FF * 2) + nt * WROW + p * 16 - i * 1024);
  }
}
__device__ __forceinline__ void g1_issue(const Args& A, const G1Ld& L, int s, int q, unsigned lds) {
  const int sr = s + L.rot >= 10 ? s + L.rot - 10 : s + L.rot;
  const char* xb = (const char*)A.x + sr * 128 + q * (32 * DM * 2);
  const char* wb = (const char*)A.w1 + (size_t)(sr * 64 + q * 16) * (FF * 2);
  const unsigned slot = lds + L_RING + (s & 3) * SBY;
  set_m0(slot + q * 4096);
  sfor<4>([&](auto I) __attribute__((always_inline)) { glds16o<I * 1024>(L.vx[I], xb); });
  set_m0(slot + XB + q * 3072);
  sfor<3>([&](auto I) __attribute__((always_inline)) { glds16o<I * 1024>(L.vw[I], wb); });
}
__device__ __forceinline__ void g1_load(const G1Ld& L, const Args& A, int lw, unsigned lds) {
  const unsigned ctl = lds + L_CTL;
#pragma unroll 1
  for (int s = 0; s < 4; ++s) g1_issue(A, L, s, lw, lds);
  vmwait<21>(); count(ctl + C_FS);
  vmwait<14>(); count(ctl + C_FS + 4);
  vmwait<7>(); count(ctl + C_FS + 8);
#pragma unroll 1
  for (int s = 4; s < 10; ++s) {
    wait4(ctl + C_FREE1, s - 3);
    g1_issue(A, L, s, lw, lds);
    vmwait<7>(); count(ctl + C_FS + (s - 1) * 4);
  }
}

template <int W>
__device__ __forceinline__ void g1_main(const Args& A, v16f (&acc)[3], v4f (&cs)[3], v4u& stat, const char* statp, unsigned statoff, bool fused,
                                        int lane, unsigned lds) {
  const int hi = lane >> 5, G = lane >> 4, q = (lane & 15) >> 2, p = lane & 3;
  unsigned xa[4], wa;
  {
    const int row = 32 * W + (lane & 31), sw = (row >> 1) & 7;
#pragma unroll
    for (int kk = 0; kk < 4; ++kk) xa[kk] = lds + L_RING + row * 128 + (((2 * kk + hi) ^ sw) * 16);
    wa = lds + L_RING + XB + (8 * hi + q) * WROW + (G & 1) * 32 + p * 8;
  }
  const unsigned full = lds + L_CTL + C_FS, freew = lds + L_CTL + C_FREE1 + W * 4;
  v4u a[2][3], b[4];
  const unsigned one2 = (lane & 15) == (G & 1) ? 0x3f803f80u : 0u;
  const v4u sel = {one2, one2, one2, one2};
  auto rda = [&](int set, int kk, int t) {
    const unsigned ad = wa + kk * 16 * WROW + t * 64;
    a[set][t] = tr2(ad, ad + 4 * WROW);
  };
  auto step = [&](int kc, int kn) {
    b[kn] = lds128(xa[kn]);
#pragma unroll
    for (int t = 0; t < 3; ++t) {
      acc[t] = mfma32(a[kc & 1][t], b[kc], acc[t]);
      if (kc == W) cs[t] = mfma16(a[kc & 1][t], sel, cs[t]);
      rda(kc & 1, kn, t);
      SB0();
    }
  };
  wait1(full, 4);
#pragma unroll
  for (int kk = 0; kk < 4; ++kk) b[kk] = v4u{0, 0, 0, 0};
#pragma unroll
  for (int kk = 0; kk < 2; ++kk) {
    b[kk] = lds128(xa[kk]);
#pragma unroll
    for (int t = 0; t < 3; ++t) rda(kk, kk, t);
  }
  SB0();
#pragma unroll 1
  for (int s = 0; s < 10; ++s) {
    if (RARE(s == 7)) { if (fused) gld128<true>(stat, statoff, statp); else gld128<false>(stat, statoff, statp); }
    const unsigned nx = full + (s < 9 ? s + 1 : 9) * 4;
    const unsigned fw = look1(nx);
    step(0, 2);
    step(1, 3);
    post(freew, s + 1);
    if (RARE(rfl(fw) < 4)) wait1(nx, 4);
    const unsigned delta = (s & 3) == 3 ? (unsigned)(-3 * SBY) : (unsigned)SBY;
#pragma unroll
    for (int kk = 0; kk < 4; ++kk) xa[kk] += delta;
    wa += delta;
    SB0();
    step(2, 0);
    step(3, 1);
  }
}

template <bool FUSED>
__device__ __forceinline__ void g1_compute(const Args& A, Tag tag, int mt, int nt, int w, int lane, unsigned lds) {
  const int tk = lane & 31, hi = lane >> 5;
  unsigned bb[3] = {0, 0, 0};
#pragma unroll
  for (int t = 0; t < 3; ++t) gld16(bb[t], (unsigned)((nt * 96 + 32 * t + tk) * 2), A.b1);
  v4u stat = {0, 0, 0, 0};
  const unsigned statoff = WS_STATS + (mt * 128 + 32 * w + tk) * 16;
  v16f acc[3];
  v4f cs[3] = {};
#pragma unroll
  for (int t = 0; t < 3; ++t)
#pragma unroll
    for (int e = 0; e < 16; ++e) acc[t][e] = 0.f;
  if (w == 0) g1_main<0>(A, acc, cs, stat, A.ws, statoff, FUSED, lane, lds);
  else if (w == 1) g1_main<1>(A, acc, cs, stat, A.ws, statoff, FUSED, lane, lds);
  else if (w == 2) g1_main<2>(A, acc, cs, stat, A.ws, statoff, FUSED, lane, lds);
  else g1_main<3>(A, acc, cs, stat, A.ws, statoff, FUSED, lane, lds);
  if ((lane & 15) < 2) {
#pragma unroll
    for (int t = 0; t < 3; ++t) st128(lds + L_CS + (w * 96 + 32 * t + 16 * (lane & 15) + 4 * (lane >> 4)) * 4, __builtin_bit_cast(v4u, cs[t]));
  }
  post(lds + L_CTL + C_CS + w * 4, 1);
  vmwait<0>();
  PIN(stat); PIN(bb[0]); PIN(bb[1]); PIN(bb[2]);
  const rsrc_t ws = mkrs(A.ws, WS_USED);
  if (FUSED) {
    for (int i = 0; i < GSPIN; ++i) {
      if (all_lanes(stat[0] == tag.a && stat[2] == tag.b)) break;
      stat = bld128<true>(ws, statoff);
    }
  }
  const unsigned nm = (stat[1] & 0xffffu) ^ 0x8000u, sdb = stat[1] >> 16;
  const v4u baug = {nm | (nm << 16), nm | (sdb << 16), 0, 0};
  const float rstd = 1.0f / bfhi(stat[1]);
  wait4(lds + L_CTL + C_CS, 1);
#pragma unroll
  for (int t = 0; t < 3; ++t) {
    const unsigned ca = lds + L_CS + (32 * t + tk) * 4;
    const float c = (lds32f(ca) + lds32f(ca + 384)) + (lds32f(ca + 768) + lds32f(ca + 1152));
    const float c0 = rbf(c), r1 = c - c0, c1 = rbf(r1), r2 = r1 - c1;
    const unsigned a0 = (f2u(c0) >> 16) | (f2u(c1) & 0xffff0000u), a1 = (pk_bf16(r2, 0.f) & 0xffffu) | (bb[t] << 16);
    acc[t] = mfma32(v4u{hi ? 0u : a0, hi ? 0u : a1, 0, 0}, baug, acc[t]);
  }
#pragma unroll
  for (int t = 0; t < 3; ++t)
#pragma unroll
    for (int hf = 0; hf < 2; ++hf) {
      unsigned pk[4];
#pragma unroll
      for (int e = 0; e < 4; ++e) {
        const float v0 = fmaxf(acc[t][8 * hf + 2 * e], 0.f) * rstd, v1 = fmaxf(acc[t][8 * hf + 2 * e + 1], 0.f) * rstd;
        pk[e] = pk_bf16(v0 * v0, v1 * v1);
      }
      bst128<FUSED>(ws, WS_H + ((mt * 4 + w) * 96 + nt * 6 + 2 * t + hf) * 1024 + lane * 16, v4u{pk[0], pk[1], pk[2], pk[3]});
    }
  post(lds + L_CTL + C_HS + w * 4, 1);
  if (FUSED) {
    vmwait<0>();
    if (lane == 0) bst64<true>(ws, WS_HF + ((mt * 4 + w) * 2 + (nt >> 3)) * 128 + (nt & 7) * 8, v2u{tag.a, tag.b});
  }
}

struct CvIn { unsigned la[3], ai[3], cr[3], ci[3], ldt, dsk; };
__device__ __forceinline__ void conv_load(CvIn& in, const Args& A, int ch, int lane) {
#pragma unroll
  for (int p = 0; p < 3; ++p) {
    const unsigned n = (unsigned)(ch * NM + p * 64 + lane) * 2;
    gld16(in.la[p], n, A.loga); gld16(in.ai[p], n, A.aim); gld16(in.cr[p], n, A.cre); gld16(in.ci[p], n, A.cim);
  }
  gld16(in.ldt, (unsigned)ch * 2, A.ldt); gld16(in.dsk, (unsigned)ch * 2, A.dsk);
}
__device__ __forceinline__ void conv_await(CvIn& in) {
  vmwait<0>();
#pragma unroll
  for (int p = 0; p < 3; ++p) { PIN(in.la[p]); PIN(in.ai[p]); PIN(in.cr[p]); PIN(in.ci[p]); }
  PIN(in.ldt); PIN(in.dsk);
}

template <bool FUSED>
__device__ __forceinline__ void conv_channel(const CvIn& in, const Args& A, int ch, int c, int lane, unsigned lds) {
  const int t16 = lane & 15, g = lane >> 4, q4 = t16 >> 2, p4 = lane & 3;
  const unsigned tab = lds + L_TAB + c * 8192, ub = lds + L_U + c * USTR;
  const float dt = rbf(expf(bf2f(in.ldt))), dsk = bf2f(in.dsk);
  float bound = 0;
#pragma unroll
  for (int p = 0; p < 3; ++p) bound += fabsf(bf2f(in.cr[p])) + fabsf(bf2f(in.ci[p]));
  bound = rowsum16(bound);
  bound = i2f(__builtin_amdgcn_readlane(f2i(bound), 0)) + i2f(__builtin_amdgcn_readlane(f2i(bound), 16)) +
          i2f(__builtin_amdgcn_readlane(f2i(bound), 32)) + i2f(__builtin_amdgcn_readlane(f2i(bound), 48));
  bound = 2.0f * bound * dt + fabsf(dsk);
  int ex = (int)((f2u(bound) >> 23) & 0xff) - 126;
  ex = ex < -100 ? -100 : (ex > 100 ? 100 : ex);
  const float sigma = u2f((unsigned)(127 + 13 - ex) << 23), isigma = u2f((unsigned)(127 - 13 + ex) << 23);

  v4f kt[3];
#pragma unroll
  for (int a = 0; a < 3; ++a) kt[a] = v4f{0, 0, 0, 0};
  const unsigned wre = tab + lane * 32;
  const unsigned rdb = tab + (g >> 1) * 2048 + (4 * (g & 1) + q4) * 32 + p4 * 8;
  auto put = [&](unsigned base, const cf* v) {
    st128(base, v4u{pk_f16(v[0].r, v[1].r), pk_f16(v[2].r, v[3].r), pk_f16(v[4].r, v[5].r), pk_f16(v[6].r, v[7].r)});
    st128(base + 16, v4u{pk_f16(v[8].r, v[9].r), pk_f16(v[10].r, v[11].r), pk_f16(v[12].r, v[13].r), pk_f16(v[14].r, v[15].r)});
    st128(base + 2048, v4u{pk_f16(v[0].i, v[1].i), pk_f16(v[2].i, v[3].i), pk_f16(v[4].i, v[5].i), pk_f16(v[6].i, v[7].i)});
    st128(base + 2064, v4u{pk_f16(v[8].i, v[9].i), pk_f16(v[10].i, v[11].i), pk_f16(v[12].i, v[13].i), pk_f16(v[14].i, v[15].i)});
  };
  auto fetch = [&](v4u (&f)[4], unsigned base) {
#pragma unroll
    for (int m = 0; m < 4; ++m) f[m] = tr2(base + m * 512, base + m * 512 + 256);
  };
  cf v[16], C0, w1, G;
  auto setup = [&](int p) {
    const unsigned lap = p == 0 ? in.la[0] : (p == 1 ? in.la[1] : in.la[2]), aip = p == 0 ? in.ai[0] : (p == 1 ? in.ai[1] : in.ai[2]);
    const unsigned crp = p == 0 ? in.cr[0] : (p == 1 ? in.cr[1] : in.cr[2]), cip = p == 0 ? in.ci[0] : (p == 1 ? in.ci[1] : in.ci[2]);
    const float a2 = -rbf(expf(bf2f(lap))) * dt * 1.44269504088896341f, brev = bf2f(aip) * dt * 0.15915494309189535f;
    const float cs = 2.0f * sigma * dt;
    C0 = cf{bf2f(crp) * cs, bf2f(cip) * cs};
    w1 = cexpp(a2, brev, 1.f); G = cexpp(a2, brev, 128.f);
    const cf w8 = cexpp(a2, -brev, 8.f);
    v[0] = cf{1.f, 0.f}; v[1] = w8;
#pragma unroll
    for (int j = 2; j < 16; ++j) v[j] = cmul(v[j - 1], w8);
  };
  v4u yf[4], af[4];
  setup(0);
  put(wre + 4096, v);
  fetch(yf, rdb + 4096);
  SB0();
#pragma unroll 1
  for (int p = 0; p < 3; ++p) {
    cf e[8];
    e[0] = cf{1.f, 0.f}; e[1] = w1;
#pragma unroll
    for (int k = 2; k < 8; ++k) e[k] = cmul(e[k - 1], w1);
    cf H = C0;
#pragma unroll
    for (int h = 0; h < 2; ++h) {
      v[8 * h] = H;
#pragma unroll
      for (int k = 1; k < 8; ++k) v[8 * h + k] = cmul(H, e[k]);
      H = cmul(H, G);
    }
    put(wre, v);
    fetch(af, rdb);
    SB0();
#pragma unroll
    for (int h = 0; h < 2; ++h) {
      v[4 * h] = H; v[4 * h + 1] = cmul(H, e[2]); v[4 * h + 2] = cmul(H, e[4]); v[4 * h + 3] = cmul(H, e[6]);
      H = cmul(H, G);
    }
#pragma unroll
    for (int h = 0; h < 4; ++h) { v[8 + 2 * h] = H; v[9 + 2 * h] = cmul(H, e[4]); H = cmul(H, G); }
    SB0();
#pragma unroll
    for (int m = 0; m < 4; ++m) kt[0] = mfma_h(af[m], yf[m], kt[0]);
    put(wre, v);
    fetch(af, rdb);
    SB0();
#pragma unroll
    for (int i = 0; i < 9; ++i) { v[i] = H; if (i < 8) H = cmul(H, G); }
#pragma unroll
    for (int i = 9; i < 16; ++i) v[i] = cf{0.f, 0.f};
    SB0();
#pragma unroll
    for (int m = 0; m < 4; ++m) kt[1] = mfma_h(af[m], yf[m], kt[1]);
    put(wre, v);
    fetch(af, rdb);
    SB0();
    if (p < 2) setup(p + 1);
    SB0();
#pragma unroll
    for (int m = 0; m < 4; ++m) kt[2] = mfma_h(af[m], yf[m], kt[2]);
    if (p < 2) { put(wre + 4096, v); fetch(yf, rdb + 4096); }
    SB0();
  }
  const unsigned kb = tab + 32;
  if (lane < 4) st64(tab + lane * 8, v2u{0, 0});
  {
    if (lane == 0) kt[0][0] += dsk * sigma;
    const int P = 4 * (g & 1) + 128 * (g >> 1) + 8 * t16;
    st64(kb + 2 * P, v2u{pk_f16(kt[0][0], kt[0][1]), pk_f16(kt[0][2], kt[0][3])});
  }
  if (g < 2) {
    const int P0 = 256 + 128 * g + 8 * t16;
    const unsigned a01 = pk_f16(kt[1][0], kt[1][1]), a23 = pk_f16(kt[1][2], kt[1][3]);
    const unsigned v0 = a01 & 0xffffu, v1 = a01 >> 16, v2 = a23 & 0xffffu, v3 = a23 >> 16;
    st128(kb + 2 * P0, v4u{v0 | (v0 << 16), v1 | (v2 << 16), v2 | (v2 << 16), v3 | (v3 << 16)});
  } else {
#pragma unroll
    for (int h = 0; h < 2; ++h) {
      const int W = 512 + 256 * (g - 2) + 128 * h + 8 * t16;
      const unsigned ab = pk_f16(kt[1][2 * h], kt[1][2 * h + 1]), a = ab & 0xffffu, b = ab >> 16;
      st128(kb + 2 * W, v4u{a | (a << 16), a | (b << 16), b | (b << 16), b | (b << 16)});
    }
  }
  unsigned k2[4];
  {
    const unsigned a = pk_f16(kt[2][0], kt[2][1]), b = pk_f16(kt[2][2], kt[2][3]);
    k2[0] = (a & 0xffffu) * 0x10001u; k2[1] = (a >> 16) * 0x10001u; k2[2] = (b & 0xffffu) * 0x10001u; k2[3] = (b >> 16) * 0x10001u;
  }
  if (g < 2) {
#pragma unroll
    for (int e = 0; e < 4; ++e) {
      const int P = 1024 + 128 * (4 * g + e) + 8 * t16;
      if (P != 1024) st64(kb + 2 * (P - 4), v2u{k2[e], k2[e]});
      st64(kb + 2 * P, v2u{k2[e], k2[e]});
    }
  } else if (lane == 32) st64(kb + 2 * 2044, v2u{k2[0], k2[0]});
  if (g < 2) {
    const int P0 = 256 + 128 * g + 8 * t16;
    if (P0 != 256) st16(kb + 2 * (P0 - 1), pk_f16(kt[1][0], 0.f));
#pragma unroll
    for (int e = 0; e < 4; ++e) {
      const int P = 1024 + 128 * (4 * g + e) + 8 * t16;
      if (!(t16 & 1)) st16(kb + 2 * (P + 4), k2[e]);
    }
    if (lane == 0) st32(kb + 2 * 1022, k2[0]);
  } else {
#pragma unroll
    for (int h = 0; h < 2; ++h) {
      const int W = 512 + 256 * (g - 2) + 128 * h + 8 * t16;
      const unsigned a = (pk_f16(kt[1][2 * h], 0.f) & 0xffffu) * 0x10001u;
      if (W != 512) st32(kb + 2 * (W - 2), a); else st16(kb + 2 * 511, a);
    }
  }
  v4u T[16];
  {
    const unsigned ta = kb + 2 * (112 - 4 * (8 * g + q4) + 4 * p4);
#pragma unroll
    for (int s = 0; s < 16; ++s) T[s] = tr2(ta + 256 * s, ta + 256 * s - 32);
  }
  wait1(lds + L_CTL + C_U, 1);
  __builtin_amdgcn_s_setprio(2);
  v4f acc[8];
#pragma unroll
  for (int C = 0; C < 8; ++C) acc[C] = v4f{0, 0, 0, 0};
  const int ord = 4 * t16 + g, po = (ord + 63) & 63;
  const int psrc = (16 * (po & 3) + (po >> 2)) * 4;
  const unsigned ua = ub + 2 * (64 - 28 - 32 + 4 * t16 + 8 * g);
  const rsrc_t ws = mkrs(A.ws, WS_USED);
  const unsigned so = WS_S + ((t16 >> 2) * DM + ch) * 128 + (16 * (t16 & 3) + 4 * g) * 2;
  v2u Bl[4], Bh[4];
  float tp[8];
  auto baddr = [&](int n) { return ua + (3 - (n >> 4)) * UPH + 64 * (n & 15); };
  auto carry = [&](int C) { tp[C] = i2f(__builtin_amdgcn_ds_bpermute(psrc, f2i(acc[C][3]))); };
  auto shift = [&](int C) {
    const float first = lane == 0 ? (C == 0 ? 0.f : tp[C > 0 ? C - 1 : 0]) : tp[C];
    float a0 = acc[C][0], a1 = acc[C][1], a2 = acc[C][2];
    asm("" : "+v"(a0), "+v"(a1), "+v"(a2));
    acc[C] = v4f{first, a0, a1, a2};
  };
  auto emit = [&](int C) {
    const v4f y = acc[C];
    bst64<FUSED>(ws, so, v2u{pk_bf16(y[0] * isigma, y[1] * isigma), pk_bf16(y[2] * isigma, y[3] * isigma)}, 4 * C * DM * 128);
  };
#pragma unroll
  for (int n = 0; n < 3; ++n) { Bl[n] = lds64(baddr(n)); SB0(); Bh[n] = lds64(baddr(n) + 8); SB0(); }
  sfor<64>([&](auto N) __attribute__((always_inline)) {
    constexpr int n = N, r = 3 - (n >> 4), q = (n & 15) - 1, c0 = (q + 1) / 2;
    const v4u B = {Bl[n & 3][0], Bl[n & 3][1], Bh[n & 3][0], Bh[n & 3][1]};
    if (n == 48) __builtin_amdgcn_s_setprio(3);
    if (q == -1 && r < 3) carry(7);
    sfor<8 - c0>([&](auto M) __attribute__((always_inline)) {
      constexpr int m = M, C = c0 + m;
      if (q == -1 && r < 3 && C >= 6) shift(C);
      acc[C] = mfma_h(T[2 * C - q], B, acc[C]);
      if (n + 3 < 64) {
        if (m == 0) Bl[(n + 3) & 3] = lds64(baddr(n + 3));
        if (m == 1) Bh[(n + 3) & 3] = lds64(baddr(n + 3) + 8);
      }
      if (m == (c0 == 7 ? 0 : 1) && (q & 1) && q > 0) { if (r > 0) carry(q >> 1); else emit(q >> 1); }
      if (m == (c0 >= 6 ? 7 - c0 : 2) && (q & 1) && q >= 3 && r > 0) shift((q >> 1) - 1);
      SB0();
    });
    if (c0 == 7 && n + 3 < 64) { Bh[(n + 3) & 3] = lds64(baddr(n + 3) + 8); SB0(); }
  });
  emit(7);
  if (FUSED) {
    const Tag tag = get_tag(lds);
    vmwait<0>();
    if (lane == 0) bst64<true>(ws, WS_SF + ch * 8, v2u{tag.a, tag.b});
  }
}

__device__ __forceinline__ constexpr int w2slot(int j) { return j < 5 ? L_W2A + j * 10240 : L_W2B + (j - 5) * 10240; }
template <bool FUSED>
__device__ __forceinline__ void g2_load(const Args& A, int nt2, int rot, int lw, int lane, unsigned lds) {
  const int kh = lw & 1, par = lw >> 1;
  const char* base = (const char*)A.w2 + (size_t)kh * (768 * DM * 2);
  unsigned vo[5];
#pragma unroll
  for (int c = 0; c < 5; ++c) {
    const int idx = c * 64 + lane, pos = (idx * 6554) >> 16, pc = idx - pos * 10;
    const int row = (pos & ~7) + 4 * (pos & 1) + ((pos >> 1) & 3);
    vo[c] = (unsigned)(row * (DM * 2) + nt2 * 160 + pc * 16 - (c & 3) * 1024);
  }
  auto issue = [&](int n) {
    const int i = 2 * n + par;
    int s = i + rot; s = s >= 24 ? s - 24 : s;
    const char* src = base + (size_t)s * (32 * DM * 2);
    const unsigned slot = lds + w2slot(i >= 20 ? i - 20 : (i >= 10 ? i - 10 : i)) + kh * 5120;
    set_m0(slot);
    sfor<4>([&](auto I) __attribute__((always_inline)) { glds16o<I * 1024>(vo[I], src); });
    set_m0(slot + 4096);
    glds16o<0>(vo[4], src);
  };
  const unsigned full = lds + L_CTL + C_FULL2 + (kh * 2 + par) * 4, freew = lds + L_CTL + C_FREE2 + kh * 8;
  wait4(lds + L_CTL + C_FREE1, 7);
  issue(0);
  vmwait<5>(); count(lds + L_CTL + C_FS + 36);
#pragma unroll 1
  for (int n = 1; n < 1; ++n) {
    const int i = 2 * n + par;
    wait4(lds + L_CTL + C_FREE1, i < 5 ? 8 : (i < 7 ? 9 : 10));
    issue(n);
  }
  wait4(lds + L_CTL + C_HS, 1);
#pragma unroll 1
  for (int n = 1; n < 5; ++n) issue(n);
  post(lds + L_CTL + C_LI + lw * 4, 1);
  vmwait<20>(); post(full, 1);
#pragma unroll 1
  for (int n = 1; n < 8; ++n) {
    vmwait<15>(); post(full, n + 1);
    wait2(freew, 2 * (n - 1) + par + 1);
    issue(n + 4);
  }
  vmwait<15>(); post(full, 9);
  vmwait<10>(); post(full, 10);
  vmwait<5>(); post(full, 11);
  vmwait<0>(); post(full, 12);
}

template <int G>
__device__ __forceinline__ v4f g2_group(const v16f (&acc)[3]) {
  constexpr int t = G < 4 ? 0 : (G < 8 ? 1 : 2), o = G < 4 ? 4 * G : (G < 8 ? 4 * (G - 4) : 8 + 4 * (G - 8));
  return v4f{acc[t][o], acc[t][o + 1], acc[t][o + 2], acc[t][o + 3]};
}
template <int KH, int I>
__device__ __forceinline__ void g2_bias(v16f (&acc)[3], const v2u (&bv)[5]) {
  constexpr int G = 5 * KH + I, t = G < 4 ? 0 : (G < 8 ? 1 : 2), o = G < 4 ? 4 * G : (G < 8 ? 4 * (G - 4) : 8 + 4 * (G - 8));
  acc[t][o] = bflo(bv[I][0]); acc[t][o + 1] = bfhi(bv[I][0]); acc[t][o + 2] = bflo(bv[I][1]); acc[t][o + 3] = bfhi(bv[I][1]);
  if constexpr (I < 4) g2_bias<KH, I + 1>(acc, bv);
}
template <int KH, int I>
__device__ __forceinline__ void g2_send(const v16f (&acc)[3], unsigned dst) {
  st128(dst + I * 1024, __builtin_bit_cast(v4u, g2_group<5 * (1 - KH) + I>(acc)));
  if constexpr (I < 4) g2_send<KH, I + 1>(acc, dst);
}

template <int KH, int I>
__device__ __forceinline__ void g2_silu(const v16f (&acc)[3], unsigned exs, v4f (&r)[5]) {
  const v4f o = g2_group<5 * KH + I>(acc), e = __builtin_bit_cast(v4f, lds128(exs + I * 1024));
#pragma unroll
  for (int k = 0; k < 4; ++k) {
    const float v = o[k] + e[k];
    r[I][k] = v * __builtin_amdgcn_rcpf(1.0f + __builtin_amdgcn_exp2f(v * -1.44269504088896341f));
  }
  if constexpr (I < 4) g2_silu<KH, I + 1>(acc, exs, r);
}

template <bool FUSED>
__device__ __forceinline__ void g2_compute(const Args& A, Tag tag, int mt2, int nt2, int rot, int w, int lane, unsigned lds) {
  const int th = w & 1, kh = w >> 1, hi = lane >> 5, T32 = 2 * mt2 + th;
  if (FUSED) wait4(lds + L_CTL + C_LI, 1);
  v2u bv[5];
#pragma unroll
  for (int i = 0; i < 5; ++i) { bv[i] = v2u{0, 0}; gld64(bv[i], (unsigned)((80 * nt2 + 8 * (5 * kh + i) + 4 * hi) * 2), A.b2); }
  if (FUSED) poll_flags<1>(mkrs(A.ws, WS_USED), WS_HF + (T32 * 2 + kh) * 128 + (lane & 3) * 16, tag);
  vmwait<0>();
#pragma unroll
  for (int i = 0; i < 5; ++i) PIN(bv[i]);
  const char* hq[8];
#pragma unroll
  for (int g = 0; g < 8; ++g) {
    hq[g] = A.ws + WS_H + (size_t)(T32 * 96 + 48 * kh) * 1024 + ((rot / 3 + g) & 7) * 6144 + 3072;
    asm volatile("" : "+s"(hq[g]));
  }
  const unsigned voff = lane * 16;
  v4u hb[6][2], wa[3], wb[3];
  v16f acc[3];
#pragma unroll
  for (int t = 0; t < 3; ++t)
#pragma unroll
    for (int e = 0; e < 16; ++e) acc[t][e] = 0.f;
  if (kh == 0) g2_bias<0, 0>(acc, bv); else g2_bias<1, 0>(acc, bv);
  auto hload = [&](auto I, auto H) __attribute__((always_inline)) {
    constexpr int i = I, h = H;
    gld128o<FUSED, (i % 3) * 2048 + h * 1024 - 3072>(hb[i % 6][h], voff, hq[i / 3]);
  };
  using I0 = std::integral_constant<int, 0>;
  using I1 = std::integral_constant<int, 1>;
  sfor<6>([&](auto K) __attribute__((always_inline)) {
    constexpr int k = K;
    hb[k][0] = v4u{0, 0, 0, 0}; hb[k][1] = v4u{0, 0, 0, 0};
    hload(K, I0{}); hload(K, I1{});
  });
  const unsigned full = lds + L_CTL + C_FULL2 + kh * 8;
  const unsigned freew = lane == 0 ? lds + L_CTL + C_FREE2 + (kh * 2 + th) * 4 : lds + L_DUMMY + lane * 4;
  const unsigned wlane = lds + kh * 5120 + (2 * ((lane & 15) >> 2) + hi) * 160 + ((lane >> 4) & 1) * 32 + (lane & 3) * 8;
  unsigned wbA = wlane + L_W2A, wbB = wlane + L_W2B;
  PIN(wbA); PIN(wbB);
  constexpr int toff[3] = {0, 64, 96};
  auto wfrag = [&](unsigned a) { return tr2(a, a + 8 * 160); };
  unsigned fw = 0, fullv = full, one = 1;
  PIN(fullv); PIN(one);
  wait2(full, 1);
#pragma unroll
  for (int t = 0; t < 3; ++t) wa[t] = wfrag(wbA + toff[t]);
#pragma unroll
  for (int t = 0; t < 3; ++t) wb[t] = wfrag(wbA + 2560 + toff[t]);
  bump(freew, one);
  auto body = [&](auto I) __attribute__((always_inline)) {
    constexpr int i = I, k = i % 6;
    constexpr bool more = i < 23, chk = i < 22;
    constexpr int j = (i + 1) % 10;
    const unsigned nxt = j < 5 ? wbA + j * 10240 : wbB + (j - 5) * 10240;
    vmwait<(i < 19 ? 10 : i == 19 ? 8 : i == 20 ? 6 : i == 21 ? 7 : i == 22 ? 5 : 3)>();
    PIN(hb[k][0]); PIN(hb[k][1]);
    SB0();
#pragma unroll
    for (int t = 0; t < 3; ++t) {
      acc[t] = mfma32(wa[t], hb[k][0], acc[t]);
      if (more) wa[t] = wfrag(nxt + toff[t]);
      if (chk && t == 0) fw = look1(fullv + (i & 1) * 4);
      if constexpr (i < 18) { if (t == 2) hload(std::integral_constant<int, i + 6>{}, I0{}); }
      SB0();
    }
#pragma unroll
    for (int t = 0; t < 3; ++t) {
      acc[t] = mfma32(wb[t], hb[k][1], acc[t]);
      if (more) wb[t] = wfrag(nxt + 2560 + toff[t]);
      if (chk && t == 1) { if (RARE(!all_lanes(fw >= (i + 2) / 2 + 1))) wait1(full + (i & 1) * 4, (i + 2) / 2 + 1); }
      if (more && t == 2) bump(freew, one);
      if constexpr (i < 18) { if (t == 2) hload(std::integral_constant<int, i + 6>{}, I1{}); }
      SB0();
    }
  };
  sfor<21>(body);
  v4u xv[3];
  auto xoff = [&](int i) {
    const int idx = min(i * 64 + lane, 159);
    return (unsigned)((T32 * 32 + idx / 5) * (DM * 2) + nt2 * 160 + kh * 80 + (idx % 5) * 16);
  };
#pragma unroll
  for (int i = 0; i < 3; ++i) { xv[i] = v4u{0, 0, 0, 0}; gld128<false>(xv[i], xoff(i), A.x); }
  SB0();
  sfor<3>([&](auto K) __attribute__((always_inline)) { body(std::integral_constant<int, 21 + K>{}); });
  wait4(lds + L_CTL + C_FREE2, 20);
  const unsigned exd = lds + L_EX + w * 5120 + lane * 16, exs = lds + L_EX + (w ^ 2) * 5120 + lane * 16;
  if (kh == 0) g2_send<0, 0>(acc, exd); else g2_send<1, 0>(acc, exd);
  post(lds + L_CTL + C_RED + w * 4, 1);
  wait1(lds + L_CTL + C_RED + (w ^ 2) * 4, 1);
  v4f sg[5];
  if (kh == 0) g2_silu<0, 0>(acc, exs, sg); else g2_silu<1, 0>(acc, exs, sg);
#pragma unroll
  for (int i = 0; i < 5; ++i) PIN(sg[i]);
  wait1(lds + L_CTL + C_SS, 1);
  const int G = lane >> 4, q = (lane & 15) >> 2, p = lane & 3;
  const unsigned sa = lds + L_ST + kh * 5120 + (4 * hi + q) * 128 + (32 * th + 16 * (G & 1) + 4 * p) * 2;
  const unsigned pa = lds + L_PT + (32 * th + (lane & 31)) * PTS + kh * 80 + 8 * hi;
  v2u sv[5];
#pragma unroll
  for (int i = 0; i < 5; ++i) sv[i] = trd(sa + i * 1024);
#pragma unroll
  for (int i = 0; i < 5; ++i)
    st64(pa + i * 16, v2u{pk_bf16(bflo(sv[i][0]) * sg[i][0], bfhi(sv[i][0]) * sg[i][1]), pk_bf16(bflo(sv[i][1]) * sg[i][2], bfhi(sv[i][1]) * sg[i][3])});
  asm volatile("" ::: "memory");
  vmwait<0>();
#pragma unroll
  for (int i = 0; i < 3; ++i) {
    const int idx = min(i * 64 + lane, 159);
    const v4u pv = lds128(lds + L_PT + (32 * th + idx / 5) * PTS + kh * 80 + (idx % 5) * 16);
    PIN(xv[i]);
    v4u o;
#pragma unroll
    for (int e = 0; e < 4; ++e) o[e] = pk_bf16(bflo(xv[i][e]) + bflo(pv[e]), bfhi(xv[i][e]) + bfhi(pv[e]));
    if (i < 2 || lane < 32) *(v4u*)((char*)A.out + xoff(i)) = o;
  }
}

template <bool FUSED>
__device__ __forceinline__ void aux_wave(const Args& A, int ph, int b, int u, int ch0, int nch, int mt2, int nt2, int lane, unsigned lds) {
  const rsrc_t ws = mkrs(A.ws, WS_USED);
  v2u ep = {0, 0};
  if (FUSED) gld64s<true>(ep, WS_EP, A.ws);
  bar();
  Tag tag = {0, 0};
  if (FUSED) {
    vmwait<0>();
    PIN(ep);
    tag.a = rfl(ep[0]) ^ 0x5bd1e995u; tag.b = rfl(ep[1]) ^ 0x9e3779b9u;
    st64(lds + L_CTL + C_TAG + 8, v2u{tag.b, 0});
    st32(lds + L_CTL + C_TAG + 4, tag.a);
    post(lds + L_CTL + C_TAG, 1);
  }
  if (ph & 1) ln_publish<FUSED>(ws, u, lane, lds);
  unsigned junk = 0;
  if (FUSED) {
    vmwait<0>();
    if (lane < 8) bst64<true>(ws, WS_F1 + lane * 2048 + b * 8, v2u{tag.a, tag.b});
#pragma unroll 1
    for (int i = 0; i < 61440 / 64 / 64 + (64 == 128); ++i) {
      wait1(lds + L_CTL + C_FS + min(4 + i / 4, 9) * 4, 4);
      gld32(junk, (unsigned)((b >> 3) * 61440 + min(i * 64 + lane, 61440 / 64 - 1) * 64), A.w2);
    }
    wait1(lds + L_CTL + C_FS + 8 * 4, 4);
    poll_flags<2>(ws, WS_F1 + (b & 7) * 2048 + lane * 16, tag);
    vmwait<0>();
    PIN(junk);
    if (b == 0 && lane == 0) {
      const u64 e = (((u64)ep[1] << 32) | ep[0]) + 1;
      bst64<true>(ws, WS_EP, v2u{(unsigned)e, (unsigned)(e >> 32)});
    }
  }
  UIn ui;
  if (ph & 2) u_load<FUSED>(ui, ws, ch0, nch, lane);
  if (ph & 2) u_store(ui, nch, lane, lds);
  if (FUSED) {
    wait4(lds + L_CTL + C_FREE2, 17);
    poll_flags<1>(ws, WS_SF + nt2 * 640 + min(lane, 39) * 16, tag);
  }
  if (ph & 4) s_stage<FUSED>(ws, mt2, nt2, lane, lds);
}

template <int PH>
__global__ __launch_bounds__(768, 3) void s4d_mlp(Args A) {
  constexpr bool FUSED = PH == 7;
  extern __shared__ __attribute__((aligned(16))) char smem[];
  const unsigned lds = (unsigned)(size_t)(LAS char*)smem;
  const int lane = threadIdx.x & 63, wv = __builtin_amdgcn_readfirstlane(threadIdx.x >> 6);
  const int b = blockIdx.x, xcd = b & 7, j = b >> 3;
  const int mt = 4 * (xcd >> 1) + (j >> 3), nt = 8 * (xcd & 1) + (j & 7);
  const int mt2 = 4 * xcd + (j >> 3), nt2 = j & 7;
  const int rot = (3 * (j >> 3) + 12 * (xcd & 1)) % 24;
  const int ch0 = 80 * xcd + 5 * (j >> 1) + 3 * (j & 1), nch = 3 - (j & 1);
  if (wv < 4) {
    LnIn in;
    if (PH & 1) ln_load(in, A, 16 * mt + nt, wv, lane);
    if (wv == 0) { st32(lds + L_CTL + lane * 4, 0); block_sync(); } else bar();
    Tag tag = {0, 0};
    if (PH & 1) tag = ln_compute<FUSED>(in, A, 16 * mt + nt, wv, lane, lds);
    if (PH & 2) g1_compute<FUSED>(A, tag, mt, nt, wv, lane, lds);
    if (PH & 4) g2_compute<FUSED>(A, tag, mt2, nt2, rot, wv, lane, lds);
  } else if (wv < 8) {
    G1Ld L;
    g1_addr(L, mt, nt, lane);
    bar();
    if (PH & 2) g1_load(L, A, wv - 4, lds);
    if (PH & 4) g2_load<FUSED>(A, nt2, rot, wv - 4, lane, lds);
  } else if (wv < 11) {
    const int c = wv - 8, ch = ch0 + c;
    const bool on = (PH & 2) && c < nch;
    CvIn in = {};
    if (on) conv_load(in, A, ch, lane);
    bar();
    conv_await(in);
    if (on) conv_channel<FUSED>(in, A, ch, c, lane, lds);
  } else {
    aux_wave<FUSED>(A, PH, b, 16 * mt + nt, ch0, nch, mt2, nt2, lane, lds);
  }
}

#if defined(__HIP_DEVICE_COMPILE__)
#pragma clang attribute pop
#endif

extern "C" void kernel_launch(void* const* d_in, const int* in_sizes, int n_in, void* d_out, int out_size, void* d_ws, size_t ws_size, hipStream_t stream) {
  Args A;
  A.x = (const unsigned short*)d_in[0]; A.loga = (const unsigned short*)d_in[1]; A.aim = (const unsigned short*)d_in[2];
  A.cre = (const unsigned short*)d_in[3]; A.cim = (const unsigned short*)d_in[4]; A.ldt = (const unsigned short*)d_in[5];
  A.dsk = (const unsigned short*)d_in[6]; A.w1 = (const unsigned short*)d_in[8]; A.b1 = (const unsigned short*)d_in[9];
  A.w2 = (const unsigned short*)d_in[10]; A.b2 = (const unsigned short*)d_in[11];
  A.out = (unsigned short*)d_out; A.ws = (char*)d_ws;
  (void)hipFuncSetAttribute((const void*)s4d_mlp<7>, hipFuncAttributeMaxDynamicSharedMemorySize, L_END);
  s4d_mlp<7><<<256, 768, L_END, stream>>>(A);
}
```
